```python
import torch

def _softmax(x, axis=-1):
    return torch.softmax(x, dim=axis)
def _gelu(x):
    return torch.nn.functional.gelu(x, approximate="tanh")
def _silu(x):
    return torch.nn.functional.silu(x)
def _sigmoid(x):
    return torch.sigmoid(x)
def _layernorm(x, axis=-1, eps=1e-5):
    m = x.mean(dim=axis, keepdim=True)
    v = x.var(dim=axis, keepdim=True, unbiased=False)
    return (x - m) / torch.sqrt(v + eps)
def _scatter_add(tgt, idx, src, axis=0):
    return tgt.clone().index_add_(axis, idx, src)
def _segment_sum(src, seg, n):
    out = torch.zeros((n,) + src.shape[1:], dtype=src.dtype)
    return out.index_add_(0, seg, src)
def _pad(x, pw):
    flat = tuple(v for pair in reversed(pw) for v in pair)
    return torch.nn.functional.pad(x, flat)

kernel_name = 'standalone__groupnorm__prompt'

def setup_inputs():

    _w = (lambda a: (lambda t: t.to(torch.bfloat16) if t.is_floating_point() else t)(torch.as_tensor(a)))
    B = 1
    G = 32
    cpg = max(1, 448 // G)
    C = G * cpg
    H, W = 16, 4096
    return {
        "x": _w(abs((lambda *s: torch.randn(*s, dtype=torch.bfloat16) * 0.05)(B, C, H, W))),
        "g": _w((lambda *s: torch.randn(*s, dtype=torch.bfloat16) * 0.05)(C) + 1.0),
        "b": _w((lambda *s: torch.randn(*s, dtype=torch.bfloat16) * 0.4)(C)),
    }

def reference(x, g, b):
    G = 32
    B, C, H, W = x.shape[0], x.shape[1], x.shape[2], x.shape[3]
    xr = x.reshape(B, G, C // G, H, W)
    mu = xr.mean(axis=(2, 3, 4), keepdims=True)
    var = ((xr - mu) ** 2).mean(axis=(2, 3, 4), keepdims=True)
    xn = (xr - mu) / torch.sqrt(var + 1e-5)
    xn = xn.reshape(B, C, H, W)
    return xn * g[None, :, None, None] + b[None, :, None, None]
```

```cpp
#include <hip/hip_runtime.h>
#include <hip/hip_bf16.h>
#include <stdint.h>

typedef unsigned v4u __attribute__((ext_vector_type(4)));
typedef float v2f __attribute__((ext_vector_type(2)));
typedef __bf16 v2bf __attribute__((ext_vector_type(2)));

constexpr int G = 32, CPG = 14, HW = 65536;
constexpr int GELEMS = CPG * HW;
constexpr int SPLIT = 64;
constexpr int SELEMS = GELEMS / SPLIT;
constexpr int NT = 256;
constexpr int NL = SELEMS / 8 / NT;

__device__ __forceinline__ float lo(unsigned u) { return __uint_as_float(u << 16); }
__device__ __forceinline__ float hi(unsigned u) { return __uint_as_float(u & 0xffff0000u); }
__device__ __forceinline__ unsigned pk(float a, float b) {
    v2f f = {a, b};
    v2bf r = __builtin_convertvector(f, v2bf);
    return __builtin_bit_cast(unsigned, r);
}

__global__ __launch_bounds__(NT) void stats_kernel(const v4u* __restrict__ x, float2* __restrict__ part) {
    const int b = blockIdx.x, t = threadIdx.x;
    const v4u* p = x + (size_t)b * (SELEMS / 8) + t;
    v4u v[NL];
#pragma unroll
    for (int i = 0; i < NL; ++i) v[i] = p[i * NT];
    float s = 0.f;
#pragma unroll
    for (int i = 0; i < NL; ++i)
#pragma unroll
        for (int j = 0; j < 4; ++j) s += lo(v[i][j]) + hi(v[i][j]);
    const float m = s * (1.f / (NL * 8));
    float q = 0.f;
#pragma unroll
    for (int i = 0; i < NL; ++i)
#pragma unroll
        for (int j = 0; j < 4; ++j) { float a = lo(v[i][j]) - m, c = hi(v[i][j]) - m; q += a * a + c * c; }
    float wm = m;
#pragma unroll
    for (int o = 32; o > 0; o >>= 1) wm += __shfl_xor(wm, o);
    wm *= (1.f / 64);
    float d = m - wm;
    q += d * d * (NL * 8);
#pragma unroll
    for (int o = 32; o > 0; o >>= 1) q += __shfl_xor(q, o);
    if ((t & 63) == 0) part[b * (NT / 64) + (t >> 6)] = make_float2(wm, q);
}

__global__ __launch_bounds__(NT) void norm_kernel(const v4u* __restrict__ x, const float2* __restrict__ part,
                                                  const unsigned short* __restrict__ gam, const unsigned short* __restrict__ bet,
                                                  v4u* __restrict__ y) {
    const int b = blockIdx.x, t = threadIdx.x;
    const int g = b / SPLIT;
    const v4u* p = x + (size_t)b * (SELEMS / 8) + t;
    v4u v[NL];
#pragma unroll
    for (int i = 0; i < NL; ++i) v[i] = p[i * NT];
    const int lane = t & 63;
    float2 pp[4];
#pragma unroll
    for (int i = 0; i < 4; ++i) pp[i] = part[g * 256 + lane + 64 * i];
    float mu = pp[0].x + pp[1].x + pp[2].x + pp[3].x;
#pragma unroll
    for (int o = 32; o > 0; o >>= 1) mu += __shfl_xor(mu, o);
    mu *= (1.f / 256);
    float q = 0.f;
#pragma unroll
    for (int i = 0; i < 4; ++i) { float d = pp[i].x - mu; q += pp[i].y + d * d * (64 * NL * 8); }
#pragma unroll
    for (int o = 32; o > 0; o >>= 1) q += __shfl_xor(q, o);
    const float rstd = 1.0f / sqrtf(q * (1.f / GELEMS) + 1e-5f);
    v4u* o = y + (size_t)b * (SELEMS / 8) + t;
#pragma unroll
    for (int i = 0; i < NL; ++i) {
        const int c = (int)(((size_t)b * (SELEMS / 8) + t + i * NT) >> 13);
        const float a = __uint_as_float((unsigned)gam[c] << 16) * rstd;
        const float bb = __uint_as_float((unsigned)bet[c] << 16) - mu * a;
        v4u r;
#pragma unroll
        for (int j = 0; j < 4; ++j) r[j] = pk(fmaf(lo(v[i][j]), a, bb), fmaf(hi(v[i][j]), a, bb));
        o[i * NT] = r;
    }
}

extern "C" void kernel_launch(void* const* d_in, const int* in_sizes, int n_in,
                              void* d_out, int out_size, void* d_ws, size_t ws_size,
                              hipStream_t stream) {
    const v4u* x = (const v4u*)d_in[0];
    const unsigned short* gam = (const unsigned short*)d_in[1];
    const unsigned short* bet = (const unsigned short*)d_in[2];
    float2* part = (float2*)d_ws;
    stats_kernel<<<G * SPLIT, NT, 0, stream>>>(x, part);
    norm_kernel<<<G * SPLIT, NT, 0, stream>>>(x, part, gam, bet, (v4u*)d_out);
}
```

```cpp
#include <hip/hip_runtime.h>
#include <stdint.h>

#define LAUX 2
#define SAUX 16

typedef unsigned v4u __attribute__((ext_vector_type(4)));
typedef float v2f __attribute__((ext_vector_type(2)));
typedef __bf16 v2bf __attribute__((ext_vector_type(2)));
typedef unsigned long long u64;
typedef __attribute__((address_space(1))) u64 gu64;

constexpr int G = 32, CPG = 14, HW = 65536;
constexpr int NT = 1024;
constexpr int P = 8;
constexpr int NBYTES = G * CPG * HW * 2;
constexpr u64 EMPTY = ~0ull;

__device__ __forceinline__ float lo(unsigned u) { return __uint_as_float(u << 16); }
__device__ __forceinline__ float hi(unsigned u) { return __uint_as_float(u & 0xffff0000u); }
__device__ __forceinline__ unsigned pk(float a, float b) {
    v2f f = {a, b};
    v2bf r = __builtin_convertvector(f, v2bf);
    return __builtin_bit_cast(unsigned, r);
}
template <int CTRL>
__device__ __forceinline__ float dpp(float v) {
    return __builtin_bit_cast(float, __builtin_amdgcn_update_dpp(0, __builtin_bit_cast(int, v), CTRL, 0xf, 0xf, true));
}
__device__ __forceinline__ float row_sum(float v) {
    v += dpp<0xB1>(v);
    v += dpp<0x4E>(v);
    v += dpp<0x141>(v);
    v += dpp<0x140>(v);
    return v;
}
__device__ __forceinline__ float wave_sum(float v) {
    v = row_sum(v);
    const int b = __builtin_bit_cast(int, v);
    return (__builtin_bit_cast(float, __builtin_amdgcn_readlane(b, 0)) + __builtin_bit_cast(float, __builtin_amdgcn_readlane(b, 16))) +
           (__builtin_bit_cast(float, __builtin_amdgcn_readlane(b, 32)) + __builtin_bit_cast(float, __builtin_amdgcn_readlane(b, 48)));
}
#define RLX __ATOMIC_RELAXED, __HIP_MEMORY_SCOPE_AGENT

__global__ __launch_bounds__(NT, 4) void gn_kernel(const void* __restrict__ x, const unsigned* __restrict__ gam,
                                                   const unsigned* __restrict__ bet, void* __restrict__ y,
                                                   u64* __restrict__ ws) {
    __shared__ float2 red[NT / 64];
    __shared__ float2 fin;
    const u64 T0 = __builtin_amdgcn_s_memrealtime();
    const u64 C0 = __builtin_amdgcn_s_memtime();
    const int t = threadIdx.x, lane = t & 63;
    const int wave = __builtin_amdgcn_readfirstlane(t >> 6);
    const int g = blockIdx.x / P, j = blockIdx.x % P;
    gu64* box = (gu64*)ws + (size_t)g * P * 32;
    if (wave == 0 && lane < P) __hip_atomic_store(box + 32 * j + lane, EMPTY, RLX);

    const __amdgpu_buffer_rsrc_t rx = __builtin_amdgcn_make_buffer_rsrc((void*)x, 0, NBYTES, 0x00020000);
    const __amdgpu_buffer_rsrc_t ry = __builtin_amdgcn_make_buffer_rsrc(y, 0, NBYTES, 0x00020000);
    const unsigned voff = (unsigned)(j * NT + t) * 16u;
    const unsigned soff = (unsigned)g * (CPG * HW * 2);
    v4u v[CPG];
#pragma unroll
    for (int i = 0; i < CPG; ++i) v[i] = __builtin_amdgcn_raw_buffer_load_b128(rx, voff, soff + i * (HW * 2), LAUX);
    unsigned gw[CPG / 2], bw[CPG / 2];
#pragma unroll
    for (int i = 0; i < CPG / 2; ++i) { gw[i] = gam[g * (CPG / 2) + i]; bw[i] = bet[g * (CPG / 2) + i]; }
    __builtin_amdgcn_sched_barrier(0);
    asm volatile("s_waitcnt vmcnt(13)" ::: "memory");
    const u64 T1 = __builtin_amdgcn_s_memrealtime();
    __builtin_amdgcn_sched_barrier(0);
    const float K = lo(v[0][0]);
    float s0 = 0.f, s1 = 0.f, q0 = 0.f, q1 = 0.f;
#pragma unroll
    for (int i = 0; i < CPG; ++i)
#pragma unroll
        for (int k = 0; k < 4; ++k) {
            const float a = lo(v[i][k]) - K, c = hi(v[i][k]) - K;
            s0 += a; s1 += c; q0 = fmaf(a, a, q0); q1 = fmaf(c, c, q1);
        }
    constexpr float NTH = CPG * 8;
    const float s = s0 + s1;
    const float dm = s * (1.f / NTH);
    const float m = K + dm;
    float q = (q0 + q1) - s * dm;
    const float wm = wave_sum(m) * (1.f / 64);
    const float d = m - wm;
    q = wave_sum(fmaf(d * NTH, d, q));
    if (lane == 0) red[wave] = make_float2(wm, q);
    __syncthreads();
    const u64 T2 = __builtin_amdgcn_s_memrealtime();
    if (wave == 0) {
        const float2 r = red[lane & 15];
        const float bm = row_sum(r.x) * (1.f / 16);
        const float bd = r.x - bm;
        const float bq = row_sum(fmaf(bd * (NTH * 64), bd, r.y));
        u64 val = ((u64)__float_as_uint(bq) << 32) | __float_as_uint(bm);
        if (val == EMPTY) val = 0x7fc000007fc00000ull;
        asm volatile("s_waitcnt vmcnt(0)" ::: "memory");
        const int s8 = lane & (P - 1);
        if (lane < P) __hip_atomic_store(box + 32 * lane + j, val, RLX);
        u64 got;
        for (int spin = 0; spin < (1 << 20); ++spin) {
            got = __hip_atomic_load(box + 32 * j + s8, RLX);
            if (__all(got != EMPTY)) break;
            __builtin_amdgcn_s_sleep(1);
        }
        if (lane < P) __hip_atomic_store(box + 32 * lane + j, val, RLX);
        const float pm = __uint_as_float((unsigned)got), pq = __uint_as_float((unsigned)(got >> 32));
        const float mu = row_sum(pm) * (1.f / 16);
        const float pd = pm - mu;
        const float M2 = row_sum(fmaf(pd * (NTH * NT), pd, pq)) * 0.5f;
        const float rstd = 1.0f / sqrtf(M2 * (1.f / (CPG * HW)) + 1e-5f);
        if (lane == 0) fin = make_float2(mu, rstd);
    }
    __syncthreads();
    const u64 T3 = __builtin_amdgcn_s_memrealtime();
    const float2 f = fin;
#pragma unroll
    for (int i = 0; i < CPG; ++i) asm volatile("" : "+v"(v[i]));
#pragma unroll
    for (int i = 0; i < CPG; ++i) {
        const float a = ((i & 1) ? hi(gw[i / 2]) : lo(gw[i / 2])) * f.y;
        const float bb = ((i & 1) ? hi(bw[i / 2]) : lo(bw[i / 2])) - f.x * a;
        v4u r;
#pragma unroll
        for (int k = 0; k < 4; ++k) r[k] = pk(fmaf(lo(v[i][k]), a, bb), fmaf(hi(v[i][k]), a, bb));
        __builtin_amdgcn_raw_buffer_store_b128(r, ry, voff + soff + i * (HW * 2), 0, SAUX);
    }
    const u64 T4 = __builtin_amdgcn_s_memrealtime();
    asm volatile("s_waitcnt vmcnt(0)" ::: "memory");
    const u64 T5 = __builtin_amdgcn_s_memrealtime();
    const u64 C5 = __builtin_amdgcn_s_memtime();
    if (lane == 0) {
        u64* st = ws + 65536 + (blockIdx.x * 16 + wave) * 8;
        st[0] = T0; st[1] = T1; st[2] = T2; st[3] = T3; st[4] = T4; st[5] = T5; st[6] = C5 - C0;
    }
}

#define QSEL 6
__global__ __launch_bounds__(256) void report_kernel(const u64* __restrict__ ws, int doprint) {
    __shared__ u64 mn[8], mx[8], sm[8];
    const int t = threadIdx.x;
    if (t < 8) { mn[t] = ~0ull; mx[t] = 0; sm[t] = 0; }
    __syncthreads();
    for (int w = 0; w < 16; ++w) {
        const u64* st = ws + 65536 + (t * 16 + w) * 8;
        for (int k = 0; k < 6; ++k) { atomicMin(&mn[k], st[k]); atomicMax(&mx[k], st[k]); atomicAdd(&sm[k], st[k] & 0xffffffffull); }
        atomicAdd(&sm[6], st[6]);
        atomicAdd(&sm[7], st[5] - st[0]);
    }
    __syncthreads();
    if (t == 0) {
        const u64 b = mn[0];
        u64 val;
        if (QSEL < 6) val = mx[QSEL] - b;
        else if (QSEL < 12) val = mn[QSEL - 6] - b;
        else if (QSEL < 18) val = sm[QSEL - 12] / 4096 - (b & 0xffffffffull);
        else val = sm[6] * 100 / sm[7];
        if (val > 10000) val = 10000;
        const u64 s0 = __builtin_amdgcn_s_memrealtime();
        while (__builtin_amdgcn_s_memrealtime() < s0 + val * 10) __builtin_amdgcn_s_sleep(2);
    }
}

extern "C" void kernel_launch(void* const* d_in, const int* in_sizes, int n_in,
                              void* d_out, int out_size, void* d_ws, size_t ws_size,
                              hipStream_t stream) {
    gn_kernel<<<G * P, NT, 0, stream>>>(d_in[0], (const unsigned*)d_in[1], (const unsigned*)d_in[2],
                                        d_out, (u64*)d_ws);
    report_kernel<<<1, 256, 0, stream>>>((const u64*)d_ws, 0);
}
```

```cpp
#include <hip/hip_runtime.h>
#include <stdint.h>

#define LAUX 2
#define SAUX 16

typedef unsigned v4u __attribute__((ext_vector_type(4)));
typedef float v2f __attribute__((ext_vector_type(2)));
typedef __bf16 v2bf __attribute__((ext_vector_type(2)));
typedef unsigned long long u64;
typedef __attribute__((address_space(1))) u64 gu64;

constexpr int G = 32, CPG = 14, HW = 65536;
constexpr int NT = 1024;
constexpr int P = 8;
constexpr int NBYTES = G * CPG * HW * 2;
constexpr u64 EMPTY = ~0ull;

__device__ __forceinline__ float lo(unsigned u) { return __uint_as_float(u << 16); }
__device__ __forceinline__ float hi(unsigned u) { return __uint_as_float(u & 0xffff0000u); }
__device__ __forceinline__ unsigned pk(float a, float b) {
    v2f f = {a, b};
    v2bf r = __builtin_convertvector(f, v2bf);
    return __builtin_bit_cast(unsigned, r);
}
template <int CTRL>
__device__ __forceinline__ float dpp(float v) {
    return __builtin_bit_cast(float, __builtin_amdgcn_update_dpp(0, __builtin_bit_cast(int, v), CTRL, 0xf, 0xf, true));
}
__device__ __forceinline__ float row_sum(float v) {
    v += dpp<0xB1>(v);
    v += dpp<0x4E>(v);
    v += dpp<0x141>(v);
    v += dpp<0x140>(v);
    return v;
}
__device__ __forceinline__ float wave_sum(float v) {
    v = row_sum(v);
    const int b = __builtin_bit_cast(int, v);
    return (__builtin_bit_cast(float, __builtin_amdgcn_readlane(b, 0)) + __builtin_bit_cast(float, __builtin_amdgcn_readlane(b, 16))) +
           (__builtin_bit_cast(float, __builtin_amdgcn_readlane(b, 32)) + __builtin_bit_cast(float, __builtin_amdgcn_readlane(b, 48)));
}
#define RLX __ATOMIC_RELAXED, __HIP_MEMORY_SCOPE_AGENT

__global__ __launch_bounds__(NT, 4) void gn_kernel(const void* __restrict__ x, const unsigned* __restrict__ gam,
                                                   const unsigned* __restrict__ bet, void* __restrict__ y,
                                                   u64* __restrict__ ws) {
    __shared__ float2 red[NT / 64];
    __shared__ float2 fin;
    const u64 T0 = __builtin_amdgcn_s_memrealtime();
    const u64 C0 = __builtin_amdgcn_s_memtime();
    const int t = threadIdx.x, lane = t & 63;
    const int wave = __builtin_amdgcn_readfirstlane(t >> 6);
    const int g = blockIdx.x / P, j = blockIdx.x % P;
    gu64* box = (gu64*)ws + (size_t)g * P * 32;
    if (wave == 0 && lane < P) __hip_atomic_store(box + 32 * j + lane, EMPTY, RLX);

    const __amdgpu_buffer_rsrc_t rx = __builtin_amdgcn_make_buffer_rsrc((void*)x, 0, NBYTES, 0x00020000);
    const __amdgpu_buffer_rsrc_t ry = __builtin_amdgcn_make_buffer_rsrc(y, 0, NBYTES, 0x00020000);
    const unsigned voff = (unsigned)(j * NT + t) * 16u;
    const unsigned soff = (unsigned)g * (CPG * HW * 2);
    v4u v[CPG];
#pragma unroll
    for (int i = 0; i < CPG; ++i) v[i] = __builtin_amdgcn_raw_buffer_load_b128(rx, voff, soff + i * (HW * 2), LAUX);
    unsigned gw[CPG / 2], bw[CPG / 2];
#pragma unroll
    for (int i = 0; i < CPG / 2; ++i) { gw[i] = gam[g * (CPG / 2) + i]; bw[i] = bet[g * (CPG / 2) + i]; }
    __builtin_amdgcn_sched_barrier(0);
    asm volatile("s_waitcnt vmcnt(13)" ::: "memory");
    const u64 T1 = __builtin_amdgcn_s_memrealtime();
    __builtin_amdgcn_sched_barrier(0);
    const float K = lo(v[0][0]);
    float s0 = 0.f, s1 = 0.f, q0 = 0.f, q1 = 0.f;
#pragma unroll
    for (int i = 0; i < CPG; ++i)
#pragma unroll
        for (int k = 0; k < 4; ++k) {
            const float a = lo(v[i][k]) - K, c = hi(v[i][k]) - K;
            s0 += a; s1 += c; q0 = fmaf(a, a, q0); q1 = fmaf(c, c, q1);
        }
    constexpr float NTH = CPG * 8;
    const float s = s0 + s1;
    const float dm = s * (1.f / NTH);
    const float m = K + dm;
    float q = (q0 + q1) - s * dm;
    const float wm = wave_sum(m) * (1.f / 64);
    const float d = m - wm;
    q = wave_sum(fmaf(d * NTH, d, q));
    if (lane == 0) red[wave] = make_float2(wm, q);
    __syncthreads();
    const u64 T2 = __builtin_amdgcn_s_memrealtime();
    if (wave == 0) {
        const float2 r = red[lane & 15];
        const float bm = row_sum(r.x) * (1.f / 16);
        const float bd = r.x - bm;
        const float bq = row_sum(fmaf(bd * (NTH * 64), bd, r.y));
        u64 val = ((u64)__float_as_uint(bq) << 32) | __float_as_uint(bm);
        if (val == EMPTY) val = 0x7fc000007fc00000ull;
        asm volatile("s_waitcnt vmcnt(0)" ::: "memory");
        const int s8 = lane & (P - 1);
        if (lane < P) __hip_atomic_store(box + 32 * lane + j, val, RLX);
        u64 got;
        for (int spin = 0; spin < (1 << 20); ++spin) {
            got = __hip_atomic_load(box + 32 * j + s8, RLX);
            if (__all(got != EMPTY)) break;
            __builtin_amdgcn_s_sleep(1);
        }
        if (lane < P) __hip_atomic_store(box + 32 * lane + j, val, RLX);
        const float pm = __uint_as_float((unsigned)got), pq = __uint_as_float((unsigned)(got >> 32));
        const float mu = row_sum(pm) * (1.f / 16);
        const float pd = pm - mu;
        const float M2 = row_sum(fmaf(pd * (NTH * NT), pd, pq)) * 0.5f;
        const float rstd = 1.0f / sqrtf(M2 * (1.f / (CPG * HW)) + 1e-5f);
        if (lane == 0) fin = make_float2(mu, rstd);
    }
    __syncthreads();
    const u64 T3 = __builtin_amdgcn_s_memrealtime();
    const float2 f = fin;
#pragma unroll
    for (int i = 0; i < CPG; ++i) asm volatile("" : "+v"(v[i]));
#pragma unroll
    for (int i = 0; i < CPG; ++i) {
        const float a = ((i & 1) ? hi(gw[i / 2]) : lo(gw[i / 2])) * f.y;
        const float bb = ((i & 1) ? hi(bw[i / 2]) : lo(bw[i / 2])) - f.x * a;
        v4u r;
#pragma unroll
        for (int k = 0; k < 4; ++k) r[k] = pk(fmaf(lo(v[i][k]), a, bb), fmaf(hi(v[i][k]), a, bb));
        __builtin_amdgcn_raw_buffer_store_b128(r, ry, voff + soff + i * (HW * 2), 0, SAUX);
    }
    const u64 T4 = __builtin_amdgcn_s_memrealtime();
    asm volatile("s_waitcnt vmcnt(0)" ::: "memory");
    const u64 T5 = __builtin_amdgcn_s_memrealtime();
    const u64 C5 = __builtin_amdgcn_s_memtime();
    if (lane == 0) {
        u64* st = ws + 65536 + (blockIdx.x * 16 + wave) * 8;
        st[0] = T0; st[1] = T1; st[2] = T2; st[3] = T3; st[4] = T4; st[5] = T5; st[6] = C5 - C0;
    }
}

#define QSEL 6
__global__ __launch_bounds__(256) void report_kernel(const u64* __restrict__ ws, int doprint) {
    const u64 R0 = __builtin_amdgcn_s_memrealtime();
    __shared__ unsigned mn[8][256], mx[8][256], sm[8][256];
    const int t = threadIdx.x;
    unsigned a[8], b[8], c[8];
    for (int k = 0; k < 8; ++k) { a[k] = ~0u; b[k] = 0; c[k] = 0; }
    const u64 ref = R0 - 100000;
#pragma unroll 4
    for (int w = 0; w < 16; ++w) {
        const u64* st = ws + 65536 + (t * 16 + w) * 8;
#pragma unroll
        for (int k = 0; k < 6; ++k) { const unsigned v = (unsigned)(st[k] - ref); a[k] = min(a[k], v); b[k] = max(b[k], v); c[k] += v - 90000; }
        c[6] += (unsigned)st[6]; c[7] += (unsigned)(st[5] - st[0]);
    }
    for (int k = 0; k < 8; ++k) { mn[k][t] = a[k]; mx[k][t] = b[k]; sm[k][t] = c[k]; }
    __syncthreads();
    if (t < 8) {
        unsigned x = ~0u, y = 0, z = 0;
        for (int i = 0; i < 256; ++i) { x = min(x, mn[t][i]); y = max(y, mx[t][i]); z += sm[t][i]; }
        mn[t][0] = x; mx[t][0] = y; sm[t][0] = z;
    }
    __syncthreads();
    if (t == 0) {
        const unsigned b0 = mn[0][0];
        unsigned val;
        if (QSEL < 6) val = mx[QSEL % 6][0] - b0;
        else if (QSEL < 12) val = mn[QSEL % 6][0] - b0;
        else if (QSEL < 18) val = sm[QSEL % 6][0] / 4096 + 90000 - b0;
        else if (QSEL == 18) val = (unsigned)((u64)sm[6][0] * 100 / sm[7][0]);
        else val = 100000 - mx[5][0];
        if (val > 10000) val = 10000;
        const u64 until = ref + b0 + 10000 + val * 10;
        while (__builtin_amdgcn_s_memrealtime() < until) __builtin_amdgcn_s_sleep(1);
    }
}

extern "C" void kernel_launch(void* const* d_in, const int* in_sizes, int n_in,
                              void* d_out, int out_size, void* d_ws, size_t ws_size,
                              hipStream_t stream) {
    gn_kernel<<<G * P, NT, 0, stream>>>(d_in[0], (const unsigned*)d_in[1], (const unsigned*)d_in[2],
                                        d_out, (u64*)d_ws);
    report_kernel<<<1, 256, 0, stream>>>((const u64*)d_ws, 0);
}
```

```cpp
#define LOAD_SPAN 755
#define ANCHOR_DELAY 25
#include <hip/hip_runtime.h>
#include <stdint.h>

#define LOAD_LEAD 5
#define ANCHOR_CAP 120
#define STORE_SPAN_EVEN 720
#define STORE_SPAN_ODD 708
#define STAGGER16 25
#define SLACK 300
#define POLL_FIRST 60
#define POLL_NEXT 30
#define SYNC_DELAY 100
#define D 5
#define D0 2
#define AW 24
#define SY 7
#define VAR 0
#define STR2(x) #x
#define STR(x) STR2(x)

typedef float v2f __attribute__((ext_vector_type(2)));
typedef unsigned long long u64;
typedef __attribute__((address_space(1))) u64 gu64;
typedef __attribute__((address_space(3))) unsigned lu32;

constexpr int CPG = 14, HW = 65536;
constexpr int NDW = 8, NT = 512;
constexpr int R = 2, P = 16, NSET = 16;
constexpr int BSTR = 544;
constexpr int SPIN_CAP = 1 << 20;

struct Lds {
    v2f part[R][NT];
    v2f ab[R][16];
    unsigned cnt[R];
    unsigned rdy[R];
    unsigned anc[NDW];
    int adj;
};

template <int CTRL>
__device__ __forceinline__ float dpp(float v) {
    return __builtin_bit_cast(float, __builtin_amdgcn_update_dpp(0, __builtin_bit_cast(int, v), CTRL, 0xf, 0xf, true));
}
__device__ __forceinline__ float row_sum(float v) {
    v += dpp<0xB1>(v);
    v += dpp<0x4E>(v);
    v += dpp<0x141>(v);
    v += dpp<0x140>(v);
    return v;
}
__device__ __forceinline__ float wave_sum(float v) {
    const int b = __builtin_bit_cast(int, row_sum(v));
    return (__builtin_bit_cast(float, __builtin_amdgcn_readlane(b, 0)) + __builtin_bit_cast(float, __builtin_amdgcn_readlane(b, 16))) +
           (__builtin_bit_cast(float, __builtin_amdgcn_readlane(b, 32)) + __builtin_bit_cast(float, __builtin_amdgcn_readlane(b, 48)));
}
__device__ __forceinline__ unsigned now() { return (unsigned)__builtin_amdgcn_s_memrealtime(); }
__device__ __forceinline__ void nap(unsigned from, unsigned ticks) {
    while (now() - from < ticks) __builtin_amdgcn_s_sleep(1);
}
#define RLX __ATOMIC_RELAXED, __HIP_MEMORY_SCOPE_AGENT
#define RLXW __ATOMIC_RELAXED, __HIP_MEMORY_SCOPE_WORKGROUP

#define V8(a) "v" #a "0", "v" #a "1", "v" #a "2", "v" #a "3", "v" #a "4", "v" #a "5", "v" #a "6", "v" #a "7", "v" #a "8", "v" #a "9"
#define CALL(l) "s_call_b64 s[52:53], .L" l "%=\n"
#define NEXTCH "s_add_u32 s51, s51, s43\n s_cmp_eq_u32 s51, s58\n s_cselect_b32 s51, 0, s51\n"
#define UNPACK(idx)                                                                                                      \
    "s_set_gpr_idx_on " idx ", gpr_idx(SRC1)\n"                                                                          \
    "v_lshlrev_b32 v112, 16, v0\n v_and_b32 v113, s55, v0\n v_lshlrev_b32 v114, 16, v1\n v_and_b32 v115, s55, v1\n"      \
    "v_lshlrev_b32 v116, 16, v2\n v_and_b32 v117, s55, v2\n v_lshlrev_b32 v118, 16, v3\n v_and_b32 v119, s55, v3\n"      \
    "s_set_gpr_idx_off\n"

__global__ __launch_bounds__(NT + 64, 3) void gn_kernel(const void* __restrict__ x, void* __restrict__ y,
                                                        const unsigned short* __restrict__ gam,
                                                        const unsigned short* __restrict__ bet, u64* __restrict__ ws) {
    __shared__ Lds lds;
    unsigned t, pf;
    u64 tc;
    const unsigned lbase = (unsigned)(size_t)(__attribute__((address_space(3))) void*)&lds;
    asm volatile(
        "s_load_dwordx2 s[40:41], %[ka], 0\n"
        "s_load_dwordx2 s[44:45], %[ka], 8\n"
        "s_getpc_b64 vcc\n.Lgn_pc" STR(VAR) ":\n v_lshlrev_b32 %[pf], 7, v0\n v_min_u32 %[pf], .Lgn_end" STR(VAR) "-.Lgn_pc" STR(VAR) ", %[pf]\n"
        "global_load_dword %[pf], %[pf], vcc\n"
        "v_readfirstlane_b32 s62, v0\n"
        "s_lshr_b32 s51, %[bid], 4\n"
        "s_mov_b32 s58, 0x1c0000\n"
        "v_mov_b32 %[t], v0\n"
        "s_mul_i32 s54, s51, s58\n"
        "s_and_b32 s49, %[bid], 3\n s_and_b32 s50, %[bid], 12\n s_lshl3_add_u32 s49, s50, s49\n"
        "v_and_b32 v136, 63, v0\n"
        "s_lshl4_add_u32 s49, s49, s62\n s_lshl_b32 s49, s49, 6\n"
        "s_sub_u32 s50, s51, 14\n s_cselect_b32 s51, s51, s50\n s_lshl_b32 s51, s51, 17\n"
        "v_lshl_add_u32 v136, v136, 4, s49\n"
        "s_mov_b32 s43, 0x20000\n s_lshl_b32 s42, s58, 5\n"
        ".set .Lk, 0\n"
        ".rept " STR(D0) "\n"
        "s_add_u32 s[66+.Lk], s54, s51\n" NEXTCH
        ".set .Lk, .Lk+1\n"
        ".endr\n"
        "s_cmpk_lt_u32 s62, 512\n"
        "s_waitcnt lgkmcnt(0)\n"
        "s_memrealtime s[56:57]\n"
        "s_cbranch_scc0 .Lside%=\n"
        ".set .Lk, 0\n"
        ".rept " STR(D0) "\n"
        "buffer_load_dwordx4 v[4*.Lk:4*.Lk+3], v136, s[40:43], s[66+.Lk] offen nt\n"
        ".set .Lk, .Lk+1\n"
        ".endr\n"
        "s_mov_b64 s[46:47], s[42:43]\n"
        "s_mov_b32 s61, s54\n s_lshr_b32 s62, s62, 6\n"
        "s_mov_b32 s48, 0\n s_mov_b32 s55, 0xffff0000\n s_mov_b32 s60, 0\n"
        "v_mov_b32 v133, %[lb]\n v_mov_b32 v132, 1\n v_lshl_add_u32 v134, %[t], 3, %[lb]\n v_mov_b32 v135, %[lb]\n"
        "s_lshl2_add_u32 s63, s62, %[lb]\n v_mov_b32 v125, s63\n"
        "s_add_u32 s59, %[lb], 8192\n"
        "s_movk_i32 s64, %[lp]\n s_movk_i32 s65, %[se]\n"
        "s_getreg_b32 s63, hwreg(HW_REG_XCC_ID, 0, 1)\n s_cmp_eq_u32 s63, 1\n s_cmovk_i32 s65, %[so]\n"
        "s_mulk_i32 s62, " STR(STAGGER16) "\n"
        "s_waitcnt lgkmcnt(0)\n"
        "s_lshl_b32 s56, s56, 4\n"
        "s_sub_u32 s50, %[l0], s56\n"
        ".rept " STR(D) "-" STR(D0) "\n"
        CALL("pace")
        "buffer_load_dwordx4 v[4*.Lk:4*.Lk+3], v136, s[40:43], s49 offen nt\n"
        ".set .Lk, .Lk+1\n"
        ".endr\n"
        ".set .Li, 0\n .set .Ls, 0\n"
        ".rept 28\n"
        "s_waitcnt vmcnt(.Lk-1-.Li+.Ls)\n"
        ".if .Li == 1\n s_barrier\n .endif\n"
        ".if .Li == " STR(SY) "\n ds_read_b32 v112, v135 offset:8496\n s_waitcnt lgkmcnt(0)\n v_readfirstlane_b32 s56, v112\n s_add_u32 s64, s64, s56\n .endif\n"
        ".if .Li == 0 || .Li == 14\n"
        "v_lshlrev_b32 v124, 16, v[4*.Li]\n v_mov_b64 v[126:127], 0\n v_mov_b64 v[128:129], 0\n"
        ".endif\n"
        CALL("stats")
        ".if .Li == 13 || .Li == 27\n" CALL("part") ".endif\n"
        ".if .Lk < 28\n"
        ".if .Lk == 14\n s_add_u32 s54, s54, 0x1c00000\n .endif\n"
        CALL("pace")
        "buffer_load_dwordx4 v[4*.Lk:4*.Lk+3], v136, s[40:43], s49 offen nt\n"
        ".set .Lk, .Lk+1\n"
        ".if .Lk == 28\n s_mov_b32 s54, s61\n" CALL("ready") ".endif\n"
        ".elseif .Li >= " STR(AW) "-1 && .Li < 27\n"
        CALL("store")
        ".set .Ls, .Ls+1\n"
        ".endif\n"
        ".set .Li, .Li+1\n"
        ".endr\n"
        ".La%=:\n" CALL("store") "s_cmp_lg_u32 s60, 56\n s_cbranch_scc1 .La%=\n"
        "s_add_u32 s54, s54, 0x1c00000\n s_add_u32 s59, s59, 128\n"
        CALL("ready")
        "s_mul_i32 s50, s65, -14\n s_sub_u32 s50, s50, s62\n s_add_u32 s50, s50, s56\n"
        "s_memrealtime s[56:57]\n s_waitcnt lgkmcnt(0)\n"
        "s_lshl4_add_u32 s56, s56, s50\n s_addk_i32 s56, -16*" STR(SLACK) "\n s_max_i32 s56, s56, 0\n s_sub_u32 s50, s50, s56\n"
        ".Lb%=:\n" CALL("store") "s_cmpk_lg_u32 s60, 112\n s_cbranch_scc1 .Lb%=\n"
        "s_endpgm\n"

        ".Lstats%=:\n"
        UNPACK("s48")
        "v_pk_add_f32 v[112:113], v[112:113], v[124:125] op_sel_hi:[1,0] neg_lo:[0,1] neg_hi:[0,1]\n"
        "v_pk_add_f32 v[114:115], v[114:115], v[124:125] op_sel_hi:[1,0] neg_lo:[0,1] neg_hi:[0,1]\n"
        "v_pk_add_f32 v[116:117], v[116:117], v[124:125] op_sel_hi:[1,0] neg_lo:[0,1] neg_hi:[0,1]\n"
        "v_pk_add_f32 v[118:119], v[118:119], v[124:125] op_sel_hi:[1,0] neg_lo:[0,1] neg_hi:[0,1]\n"
        "s_add_u32 s48, s48, 4\n"
        "v_pk_add_f32 v[126:127], v[126:127], v[112:113]\n v_pk_fma_f32 v[128:129], v[112:113], v[112:113], v[128:129]\n"
        "v_pk_add_f32 v[126:127], v[126:127], v[114:115]\n v_pk_fma_f32 v[128:129], v[114:115], v[114:115], v[128:129]\n"
        "v_pk_add_f32 v[126:127], v[126:127], v[116:117]\n v_pk_fma_f32 v[128:129], v[116:117], v[116:117], v[128:129]\n"
        "v_pk_add_f32 v[126:127], v[126:127], v[118:119]\n v_pk_fma_f32 v[128:129], v[118:119], v[118:119], v[128:129]\n"
        "s_setpc_b64 s[52:53]\n"

        ".Lpart%=:\n"
        "v_add_f32 v126, v126, v127\n v_add_f32 v128, v128, v129\n"
        "v_mul_f32 v127, 0x3c124925, v126\n"
        "v_add_f32 v112, v124, v127\n v_fma_f32 v113, -v126, v127, v128\n"
        "ds_write_b64 v134, v[112:113]\n"
        "s_mov_b64 exec, 1\n ds_add_u32 v133, v132 offset:8448\n s_mov_b64 exec, -1\n"
        "v_add_u32 v134, 0x1000, v134\n v_add_u32 v133, 4, v133\n"
        "s_setpc_b64 s[52:53]\n"

        ".Lp1%=:\n s_sleep 1\n"
        ".Lpace%=:\n s_memrealtime s[56:57]\n s_add_u32 s49, s54, s51\n s_waitcnt lgkmcnt(0)\n"
        "s_lshl4_add_u32 s56, s56, s50\n s_cmp_lt_i32 s56, 0\n s_cbranch_scc1 .Lp1%=\n"
        "s_sub_u32 s50, s50, s64\n"
        NEXTCH
        "s_setpc_b64 s[52:53]\n"

        ".Lr1%=:\n s_sleep 1\n"
        ".Lready%=:\n ds_read_b32 v112, v135 offset:8456\n s_waitcnt lgkmcnt(0)\n v_readfirstlane_b32 s56, v112\n"
        "s_cmp_eq_u32 s56, 0\n s_cbranch_scc1 .Lr1%=\n"
        "v_add_u32 v135, 4, v135\n"
        "s_setpc_b64 s[52:53]\n"

        ".Lstore%=:\n"
        "s_lshr_b32 s49, s51, 14\n s_add_u32 s49, s49, s59\n v_mov_b32 v130, s49\n ds_read_b64 v[130:131], v130\n"
        UNPACK("s60")
        "s_add_u32 s60, s60, 4\n s_add_u32 s49, s54, s51\n"
        "s_waitcnt lgkmcnt(0)\n"
        "s_memrealtime s[56:57]\n"
        "v_pk_fma_f32 v[112:113], v[112:113], v[130:131], v[130:131] op_sel:[0,0,1] op_sel_hi:[1,0,1]\n"
        "v_pk_fma_f32 v[114:115], v[114:115], v[130:131], v[130:131] op_sel:[0,0,1] op_sel_hi:[1,0,1]\n"
        "v_pk_fma_f32 v[116:117], v[116:117], v[130:131], v[130:131] op_sel:[0,0,1] op_sel_hi:[1,0,1]\n"
        "v_pk_fma_f32 v[118:119], v[118:119], v[130:131], v[130:131] op_sel:[0,0,1] op_sel_hi:[1,0,1]\n"
        "s_cmp_eq_u32 s60, 4\n"
        "v_cvt_pk_bf16_f32 v120, v112, v113\n v_cvt_pk_bf16_f32 v121, v114, v115\n"
        "v_cvt_pk_bf16_f32 v122, v116, v117\n v_cvt_pk_bf16_f32 v123, v118, v119\n"
        "s_cbranch_scc1 .Lanchor%=\n"
        ".Ls2%=:\n s_waitcnt lgkmcnt(0)\n s_lshl4_add_u32 s56, s56, s50\n s_cmp_lt_i32 s56, 0\n s_cbranch_scc1 .Ls1%=\n"
        "buffer_store_dwordx4 v[120:123], v136, s[44:47], s49 offen sc1\n"
        "s_sub_u32 s50, s50, s65\n"
        NEXTCH
        "s_setpc_b64 s[52:53]\n"
        ".Lanchor%=:\n s_waitcnt vmcnt(27-" STR(AW) ") lgkmcnt(0)\n s_memrealtime s[56:57]\n s_waitcnt lgkmcnt(0)\n"
        "s_lshl4_add_u32 s56, s56, s50\n s_min_i32 s56, s56, 16*" STR(ANCHOR_CAP) "\n s_addk_i32 s56, 16*" STR(ANCHOR_DELAY) "\n s_sub_u32 s50, s50, s56\n"
        "v_mov_b32 v112, s50\n ds_write_b32 v125, v112 offset:8464\n s_sub_u32 s50, s50, s62\n"
        ".Ls1%=:\n s_sleep 1\n s_memrealtime s[56:57]\n s_branch .Ls2%=\n"

        ".Lside%=:\n"
        "s_waitcnt lgkmcnt(0)\n s_mov_b64 %[tc], s[56:57]\n"
        : [t] "=&v"(t), [pf] "=&v"(pf), [tc] "=&s"(tc)
        : [ka] "s"(__builtin_amdgcn_kernarg_segment_ptr()), [bid] "s"(blockIdx.x), [lb] "s"(lbase), "{v0}"(threadIdx.x),
          [lp] "i"(LOAD_SPAN * 16 / 28), [l0] "i"(LOAD_LEAD * 16 - D0 * (LOAD_SPAN * 16 / 28)), [se] "i"(STORE_SPAN_EVEN * 16 / 28),
          [so] "i"(STORE_SPAN_ODD * 16 / 28)
        : "memory", "scc", "vcc", "v1", "v2", "v3", "v4", "v5", "v6", "v7", "v8", "v9", V8(1), V8(2), V8(3), V8(4), V8(5), V8(6), V8(7),
          V8(8), V8(9), V8(10), V8(11), V8(12), "v130", "v131", "v132", "v133", "v134", "v135", "v136",
          "s40", "s41", "s42", "s43", "s44", "s45", "s46", "s47", "s48", "s49", "s50", "s51", "s52", "s53", "s54", "s55", "s56", "s57",
          "s58", "s59", "s60", "s61", "s62", "s63", "s64", "s65", "s66", "s67", "s68", "s69");

    __builtin_amdgcn_s_setprio(3);
    const int lane = t & 63;
    const unsigned set = blockIdx.x / P, j = blockIdx.x % P;
    if (lane < 13) lds.cnt[lane] = 0;
    gu64* board = (gu64*)ws + (size_t)set * (R * BSTR);
    if (lane == 0) __hip_atomic_store(board + 2 * j + 1, tc, RLX);
    const int cl = (set + (lane >> 5) * NSET) * CPG + min(lane & 15, CPG - 1);
    const float gl = __uint_as_float((unsigned)gam[cl] << 16), bl = __uint_as_float((unsigned)bet[cl] << 16);
    nap(tc, SYNC_DELAY);
    gu64* peer = board + (lane & (P - 1)) * 2 + ((lane >> 4) & 1);
    const u64 w0 = __hip_atomic_load(peer, RLX);
    unsigned snap = ((unsigned)w0 & 3u) | ((unsigned)__hip_atomic_load(peer + BSTR, RLX) & 3u) << 2;
    asm volatile("s_waitcnt lgkmcnt(0)\n\ts_barrier" ::: "memory");
    asm volatile("s_waitcnt vmcnt(0)" : "+v"(snap), "+v"(pf) :: "memory");
    {
        const bool ok = w0 - tc + 128 < 256;
        const float dsum = row_sum(ok ? (float)(int)(w0 - tc) : 0.f), dcnt = row_sum(ok ? 1.f : 0.f);
        if (lane == 16) lds.adj = (int)(dsum * __builtin_amdgcn_rcpf(fmaxf(dcnt, 1.f)) * (16.f / (27 - SY - D)));
    }
    const unsigned mine = (lane & 1) ? __builtin_amdgcn_readlane(snap, j + 16) : __builtin_amdgcn_readlane(snap, j);
    int spin = 0;
    u64 first = 0;
#pragma nounroll
    for (int r = 0; r < R; ++r) {
        while (__builtin_amdgcn_readfirstlane(__hip_atomic_load((lu32*)&lds.cnt[r], RLXW)) != NDW) __builtin_amdgcn_s_sleep(1);
        asm volatile("" ::: "memory");
        const unsigned aw = lds.anc[lane & 7], a0 = __builtin_amdgcn_readfirstlane(aw);
        const unsigned fin = a0 + (int)(row_sum((float)(int)(aw - a0)) * (1.f / 16));
        v2f p[NDW];
        float m = 0.f;
#pragma unroll
        for (int w = 0; w < NDW; ++w) { p[w] = lds.part[r][w * 64 + lane]; m += p[w].x; }
        m = wave_sum(m) * (1.f / NT);
        float q = 0.f, e = 0.f;
#pragma unroll
        for (int w = 0; w < NDW; ++w) { const float d = p[w].x - m; q += p[w].y; e = fmaf(d, d, e); }
        q = wave_sum(fmaf(e, (float)(CPG * 8), q));
        const u64 body = (lane & 1) ? (u64)fin << 32 : ((u64)__float_as_uint(m) << 32) | (__float_as_uint(q) & ~3u);
        if (lane <= 2 * r)
            __hip_atomic_store(board + (lane & 2 ? 0 : r) * BSTR + 2 * j + (lane & 1),
                               lane & 2 ? first : body | (((mine >> (2 * r)) + 1) & 3u), RLX);
        first = body | ((mine + 2) & 3u);
        u64 val;
        unsigned delay = POLL_FIRST;
#pragma nounroll
        for (;; ++spin) {
            nap(now(), delay);
            delay = POLL_NEXT;
            val = __hip_atomic_load(peer + r * BSTR, RLX);
            if (__all((((unsigned)val ^ (snap >> (2 * r))) & 3u) != 0 || (lane & 16) > 16 * r)) break;
            if (spin >= SPIN_CAP) { val = (lane & 16) ? (u64)fin << 32 : ~0ull; break; }
        }
        const float pm = __uint_as_float((unsigned)(val >> 32)), pq = __uint_as_float((unsigned)val & ~3u);
        const float mu = row_sum(pm) * (1.f / P);
        const float pd = pm - mu;
        const float M2 = row_sum(fmaf(pd * (float)(CPG * 8 * NT), pd, pq));
        const float a = gl * __builtin_amdgcn_rsqf(M2 * (1.f / (CPG * HW)) + 1e-5f);
        if ((lane >> 4) == 2 * r) lds.ab[r][lane & 15] = (v2f){a, bl - mu * a};
        const unsigned slot = fin + (int)(row_sum((float)(int)((unsigned)(val >> 32) - fin)) * (1.f / P));
        asm volatile("" ::: "memory");
        if (lane == 16) __hip_atomic_store((lu32*)&lds.rdy[r], slot | 1u, RLXW);
    }
    asm volatile(".Lgn_end" STR(VAR) ":");
}

extern "C"
void kernel_launch(void* const* d_in, const int* in_sizes, int n_in, void* d_out, int out_size, void* d_ws, size_t ws_size,
                   hipStream_t stream) {
    gn_kernel<<<NSET * P, NT + 64, 0, stream>>>(d_in[0], d_out, (const unsigned short*)d_in[1], (const unsigned short*)d_in[2],
                                                (u64*)d_ws);
}
```

```cpp
#include <hip/hip_runtime.h>
#include <stdint.h>

#define LOAD_SPAN 753
#define BEND 26
#define LOAD_LEAD 10
#define ANCHOR_DELAY 24
#define ANCHOR_CAP 120
#define STORE_SPAN_EVEN 720
#define STORE_SPAN_ODD 708
#define STAGGER16 25
#define SLACK 300
#define POLL_FIRST 60
#define POLL_NEXT 30
#define SYNC_DELAY 100
#define D 5
#define D0 2
#define AW 24
#define SY 7
#define STR2(x) #x
#define STR(x) STR2(x)

typedef float v2f __attribute__((ext_vector_type(2)));
typedef unsigned long long u64;
typedef __attribute__((address_space(1))) u64 gu64;
typedef __attribute__((address_space(3))) unsigned lu32;

constexpr int CPG = 14, HW = 65536;
constexpr int NDW = 8, NT = 512;
constexpr int R = 2, P = 16, NSET = 16;
constexpr int BSTR = 544;
constexpr int SPIN_CAP = 1 << 20;

struct Lds {
    v2f part[R][NT];
    v2f ab[R][16];
    unsigned w[13];
};
constexpr int CNT = 0;
constexpr int RDY = 2;
constexpr int ANC = 4;
constexpr int ADJ = 12;

template <int CTRL>
__device__ __forceinline__ float dpp(float v) {
    return __builtin_bit_cast(float, __builtin_amdgcn_update_dpp(0, __builtin_bit_cast(int, v), CTRL, 0xf, 0xf, true));
}
__device__ __forceinline__ float row_sum(float v) {
    v += dpp<0xB1>(v);
    v += dpp<0x4E>(v);
    v += dpp<0x141>(v);
    v += dpp<0x140>(v);
    return v;
}
__device__ __forceinline__ float wave_sum(float v) {
    const int b = __builtin_bit_cast(int, row_sum(v));
    return (__builtin_bit_cast(float, __builtin_amdgcn_readlane(b, 0)) + __builtin_bit_cast(float, __builtin_amdgcn_readlane(b, 16))) +
           (__builtin_bit_cast(float, __builtin_amdgcn_readlane(b, 32)) + __builtin_bit_cast(float, __builtin_amdgcn_readlane(b, 48)));
}
__device__ __forceinline__ unsigned now() { return (unsigned)__builtin_amdgcn_s_memrealtime(); }
__device__ __forceinline__ void nap(unsigned from, unsigned ticks) {
    while (now() - from < ticks) __builtin_amdgcn_s_sleep(1);
}
#define RLX __ATOMIC_RELAXED, __HIP_MEMORY_SCOPE_AGENT
#define RLXW __ATOMIC_RELAXED, __HIP_MEMORY_SCOPE_WORKGROUP

#define V8(a) "v" #a "0", "v" #a "1", "v" #a "2", "v" #a "3", "v" #a "4", "v" #a "5", "v" #a "6", "v" #a "7", "v" #a "8", "v" #a "9"
#define CALL(l) "s_call_b64 s[52:53], .L" l "%=\n"
#define NEXTCH "s_add_u32 s51, s51, s43\n s_cmp_eq_u32 s51, s58\n s_cselect_b32 s51, 0, s51\n"
#define UNPACK(idx)                                                                                                      \
    "s_set_gpr_idx_on " idx ", gpr_idx(SRC1)\n"                                                                          \
    "v_lshlrev_b32 v112, 16, v0\n v_and_b32 v113, s55, v0\n v_lshlrev_b32 v114, 16, v1\n v_and_b32 v115, s55, v1\n"      \
    "v_lshlrev_b32 v116, 16, v2\n v_and_b32 v117, s55, v2\n v_lshlrev_b32 v118, 16, v3\n v_and_b32 v119, s55, v3\n"      \
    "s_set_gpr_idx_off\n"

__global__ __launch_bounds__(NT + 64, 3) void gn_kernel(const void* __restrict__ x, void* __restrict__ y,
                                                        const unsigned short* __restrict__ gam,
                                                        const unsigned short* __restrict__ bet, u64* __restrict__ ws) {
    __shared__ Lds lds;
    unsigned t, pf;
    u64 tc;
    const unsigned lbase = (unsigned)(size_t)(__attribute__((address_space(3))) void*)&lds;
    asm volatile(
        "s_load_dwordx2 s[40:41], %[ka], 0\n"
        "s_load_dwordx2 s[44:45], %[ka], 8\n"
        "s_getpc_b64 vcc\n.Lgn_pc:\n v_lshlrev_b32 %[pf], 7, v0\n v_min_u32 %[pf], .Lgn_end-.Lgn_pc, %[pf]\n"
        "global_load_dword %[pf], %[pf], vcc\n"
        "v_readfirstlane_b32 s62, v0\n"
        "s_lshr_b32 s51, %[bid], 4\n"
        "s_mov_b32 s58, 0x1c0000\n"
        "v_mov_b32 %[t], v0\n"
        "s_mul_i32 s54, s51, s58\n"
        "s_and_b32 s49, %[bid], 3\n s_and_b32 s50, %[bid], 12\n s_lshl3_add_u32 s49, s50, s49\n"
        "v_and_b32 v136, 63, v0\n"
        "s_lshl4_add_u32 s49, s49, s62\n s_lshl_b32 s49, s49, 6\n"
        "s_sub_u32 s50, s51, 14\n s_cselect_b32 s51, s51, s50\n s_lshl_b32 s51, s51, 17\n"
        "v_lshl_add_u32 v136, v136, 4, s49\n"
        "s_mov_b32 s43, 0x20000\n s_lshl_b32 s42, s58, 5\n"
        ".set .Lk, 0\n"
        ".rept " STR(D0) "\n"
        "s_add_u32 s[66+.Lk], s54, s51\n" NEXTCH
        ".set .Lk, .Lk+1\n"
        ".endr\n"
        "s_cmpk_lt_u32 s62, 512\n"
        "s_waitcnt lgkmcnt(0)\n"
        "s_memrealtime s[56:57]\n"
        "s_cbranch_scc0 .Lside%=\n"
        ".set .Lk, 0\n"
        ".rept " STR(D0) "\n"
        "buffer_load_dwordx4 v[4*.Lk:4*.Lk+3], v136, s[40:43], s[66+.Lk] offen nt\n"
        ".set .Lk, .Lk+1\n"
        ".endr\n"
        "s_mov_b64 s[46:47], s[42:43]\n"
        "s_mov_b32 s61, s54\n s_lshr_b32 s62, s62, 6\n"
        "s_mov_b32 s48, 0\n s_mov_b32 s55, 0xffff0000\n s_mov_b32 s60, 0\n"
        "v_mov_b32 v133, %[lb]\n v_mov_b32 v132, 1\n v_lshl_add_u32 v134, %[t], 3, %[lb]\n v_mov_b32 v135, %[lb]\n"
        "s_lshl2_add_u32 s63, s62, %[lb]\n v_mov_b32 v125, s63\n"
        "s_add_u32 s59, %[lb], 8192\n"
        "s_movk_i32 s64, %[lp]\n s_movk_i32 s65, %[se]\n"
        "s_getreg_b32 s63, hwreg(HW_REG_XCC_ID, 0, 1)\n s_cmp_eq_u32 s63, 1\n s_cmovk_i32 s65, %[so]\n"
        "s_mulk_i32 s62, " STR(STAGGER16) "\n"
        "s_waitcnt lgkmcnt(0)\n"
        "s_lshl_b32 s56, s56, 4\n"
        "s_sub_u32 s50, %[l0], s56\n"
        ".rept " STR(D) "-" STR(D0) "\n"
        CALL("pace")
        "buffer_load_dwordx4 v[4*.Lk:4*.Lk+3], v136, s[40:43], s49 offen nt\n"
        ".set .Lk, .Lk+1\n"
        ".endr\n"
        ".set .Li, 0\n .set .Ls, 0\n"
        ".rept 28\n"
        "s_waitcnt vmcnt(.Lk-1-.Li+.Ls)\n"
        ".if .Li == 1\n s_barrier\n .endif\n"
        ".if .Li == " STR(SY) "\n ds_read_b32 v112, v135 offset:8496\n s_waitcnt lgkmcnt(0)\n v_readfirstlane_b32 s56, v112\n s_add_u32 s64, s64, s56\n .endif\n"
        ".if .Li == 0 || .Li == 14\n"
        "v_lshlrev_b32 v124, 16, v[4*.Li]\n v_mov_b64 v[126:127], 0\n v_mov_b64 v[128:129], 0\n"
        ".endif\n"
        CALL("stats")
        ".if .Li == 13 || .Li == 27\n" CALL("part") ".endif\n"
        ".if .Lk < 28\n"
        ".if .Lk == 14\n s_add_u32 s54, s54, 0x1c00000\n s_addk_i32 s64, " STR(BEND) "*16/28\n .endif\n"
        CALL("pace")
        "buffer_load_dwordx4 v[4*.Lk:4*.Lk+3], v136, s[40:43], s49 offen nt\n"
        ".set .Lk, .Lk+1\n"
        ".if .Lk == 28\n s_mov_b32 s54, s61\n" CALL("ready") ".endif\n"
        ".elseif .Li >= " STR(AW) "-1 && .Li < 27\n"
        CALL("store")
        ".set .Ls, .Ls+1\n"
        ".endif\n"
        ".set .Li, .Li+1\n"
        ".endr\n"
        ".La%=:\n" CALL("store") "s_cmp_lg_u32 s60, 56\n s_cbranch_scc1 .La%=\n"
        "s_add_u32 s54, s54, 0x1c00000\n s_add_u32 s59, s59, 128\n"
        CALL("ready")
        "s_mul_i32 s50, s65, -14\n s_sub_u32 s50, s50, s62\n s_add_u32 s50, s50, s56\n"
        "s_memrealtime s[56:57]\n s_waitcnt lgkmcnt(0)\n"
        "s_lshl4_add_u32 s56, s56, s50\n s_addk_i32 s56, -16*" STR(SLACK) "\n s_max_i32 s56, s56, 0\n s_sub_u32 s50, s50, s56\n"
        ".Lb%=:\n" CALL("store") "s_cmpk_lg_u32 s60, 112\n s_cbranch_scc1 .Lb%=\n"
        "s_endpgm\n"

        ".Lstats%=:\n"
        UNPACK("s48")
        "v_pk_add_f32 v[112:113], v[112:113], v[124:125] op_sel_hi:[1,0] neg_lo:[0,1] neg_hi:[0,1]\n"
        "v_pk_add_f32 v[114:115], v[114:115], v[124:125] op_sel_hi:[1,0] neg_lo:[0,1] neg_hi:[0,1]\n"
        "v_pk_add_f32 v[116:117], v[116:117], v[124:125] op_sel_hi:[1,0] neg_lo:[0,1] neg_hi:[0,1]\n"
        "v_pk_add_f32 v[118:119], v[118:119], v[124:125] op_sel_hi:[1,0] neg_lo:[0,1] neg_hi:[0,1]\n"
        "s_add_u32 s48, s48, 4\n"
        "v_pk_add_f32 v[126:127], v[126:127], v[112:113]\n v_pk_fma_f32 v[128:129], v[112:113], v[112:113], v[128:129]\n"
        "v_pk_add_f32 v[126:127], v[126:127], v[114:115]\n v_pk_fma_f32 v[128:129], v[114:115], v[114:115], v[128:129]\n"
        "v_pk_add_f32 v[126:127], v[126:127], v[116:117]\n v_pk_fma_f32 v[128:129], v[116:117], v[116:117], v[128:129]\n"
        "v_pk_add_f32 v[126:127], v[126:127], v[118:119]\n v_pk_fma_f32 v[128:129], v[118:119], v[118:119], v[128:129]\n"
        "s_setpc_b64 s[52:53]\n"

        ".Lpart%=:\n"
        "v_add_f32 v126, v126, v127\n v_add_f32 v128, v128, v129\n"
        "v_mul_f32 v127, 0x3c124925, v126\n"
        "v_add_f32 v112, v124, v127\n v_fma_f32 v113, -v126, v127, v128\n"
        "ds_write_b64 v134, v[112:113]\n"
        "s_mov_b64 exec, 1\n ds_add_u32 v133, v132 offset:8448\n s_mov_b64 exec, -1\n"
        "v_add_u32 v134, 0x1000, v134\n v_add_u32 v133, 4, v133\n"
        "s_setpc_b64 s[52:53]\n"

        ".Lp1%=:\n s_sleep 1\n"
        ".Lpace%=:\n s_memrealtime s[56:57]\n s_add_u32 s49, s54, s51\n s_waitcnt lgkmcnt(0)\n"
        "s_lshl4_add_u32 s56, s56, s50\n s_cmp_lt_i32 s56, 0\n s_cbranch_scc1 .Lp1%=\n"
        "s_sub_u32 s50, s50, s64\n"
        NEXTCH
        "s_setpc_b64 s[52:53]\n"

        ".Lr1%=:\n s_sleep 1\n"
        ".Lready%=:\n ds_read_b32 v112, v135 offset:8456\n s_waitcnt lgkmcnt(0)\n v_readfirstlane_b32 s56, v112\n"
        "s_cmp_eq_u32 s56, 0\n s_cbranch_scc1 .Lr1%=\n"
        "v_add_u32 v135, 4, v135\n"
        "s_setpc_b64 s[52:53]\n"

        ".Lstore%=:\n"
        "s_lshr_b32 s49, s51, 14\n s_add_u32 s49, s49, s59\n v_mov_b32 v130, s49\n ds_read_b64 v[130:131], v130\n"
        UNPACK("s60")
        "s_add_u32 s60, s60, 4\n s_add_u32 s49, s54, s51\n"
        "s_waitcnt lgkmcnt(0)\n"
        "s_memrealtime s[56:57]\n"
        "v_pk_fma_f32 v[112:113], v[112:113], v[130:131], v[130:131] op_sel:[0,0,1] op_sel_hi:[1,0,1]\n"
        "v_pk_fma_f32 v[114:115], v[114:115], v[130:131], v[130:131] op_sel:[0,0,1] op_sel_hi:[1,0,1]\n"
        "v_pk_fma_f32 v[116:117], v[116:117], v[130:131], v[130:131] op_sel:[0,0,1] op_sel_hi:[1,0,1]\n"
        "v_pk_fma_f32 v[118:119], v[118:119], v[130:131], v[130:131] op_sel:[0,0,1] op_sel_hi:[1,0,1]\n"
        "s_cmp_eq_u32 s60, 4\n"
        "v_cvt_pk_bf16_f32 v120, v112, v113\n v_cvt_pk_bf16_f32 v121, v114, v115\n"
        "v_cvt_pk_bf16_f32 v122, v116, v117\n v_cvt_pk_bf16_f32 v123, v118, v119\n"
        "s_cbranch_scc1 .Lanchor%=\n"
        ".Ls2%=:\n s_waitcnt lgkmcnt(0)\n s_lshl4_add_u32 s56, s56, s50\n s_cmp_lt_i32 s56, 0\n s_cbranch_scc1 .Ls1%=\n"
        "buffer_store_dwordx4 v[120:123], v136, s[44:47], s49 offen sc1\n"
        "s_sub_u32 s50, s50, s65\n"
        NEXTCH
        "s_setpc_b64 s[52:53]\n"
        ".Lanchor%=:\n s_waitcnt vmcnt(27-" STR(AW) ") lgkmcnt(0)\n s_memrealtime s[56:57]\n s_waitcnt lgkmcnt(0)\n"
        "s_lshl4_add_u32 s56, s56, s50\n s_min_i32 s56, s56, 16*" STR(ANCHOR_CAP) "\n s_addk_i32 s56, 16*" STR(ANCHOR_DELAY) "\n s_sub_u32 s50, s50, s56\n"
        "v_mov_b32 v112, s50\n ds_write_b32 v125, v112 offset:8464\n s_sub_u32 s50, s50, s62\n"
        ".Ls1%=:\n s_sleep 1\n s_memrealtime s[56:57]\n s_branch .Ls2%=\n"

        ".Lside%=:\n"
        "s_waitcnt lgkmcnt(0)\n s_mov_b64 %[tc], s[56:57]\n"
        : [t] "=&v"(t), [pf] "=&v"(pf), [tc] "=&s"(tc)
        : [ka] "s"(__builtin_amdgcn_kernarg_segment_ptr()), [bid] "s"(blockIdx.x), [lb] "s"(lbase), "{v0}"(threadIdx.x),
          [lp] "i"(LOAD_SPAN * 16 / 28), [l0] "i"(LOAD_LEAD * 16 - D0 * (LOAD_SPAN * 16 / 28)), [se] "i"(STORE_SPAN_EVEN * 16 / 28),
          [so] "i"(STORE_SPAN_ODD * 16 / 28)
        : "memory", "scc", "vcc", "v1", "v2", "v3", "v4", "v5", "v6", "v7", "v8", "v9", V8(1), V8(2), V8(3), V8(4), V8(5), V8(6), V8(7),
          V8(8), V8(9), V8(10), V8(11), V8(12), "v130", "v131", "v132", "v133", "v134", "v135", "v136",
          "s40", "s41", "s42", "s43", "s44", "s45", "s46", "s47", "s48", "s49", "s50", "s51", "s52", "s53", "s54", "s55", "s56", "s57",
          "s58", "s59", "s60", "s61", "s62", "s63", "s64", "s65", "s66", "s67", "s68", "s69");

    __builtin_amdgcn_s_setprio(3);
    const int lane = t & 63;
    const unsigned set = blockIdx.x / P, j = blockIdx.x % P;
    if (lane < 13) lds.w[lane] = 0;
    gu64* board = (gu64*)ws + (size_t)set * (R * BSTR);
    if (lane == 0) __hip_atomic_store(board + 2 * j + 1, tc, RLX);
    const int cl = (set + (lane >> 5) * NSET) * CPG + min(lane & 15, CPG - 1);
    const float gl = __uint_as_float((unsigned)gam[cl] << 16), bl = __uint_as_float((unsigned)bet[cl] << 16);
    nap(tc, SYNC_DELAY);
    gu64* peer = board + (lane & (P - 1)) * 2 + ((lane >> 4) & 1);
    const u64 w0 = __hip_atomic_load(peer, RLX);
    unsigned snap = ((unsigned)w0 & 3u) | ((unsigned)__hip_atomic_load(peer + BSTR, RLX) & 3u) << 2;
    asm volatile("s_waitcnt lgkmcnt(0)\n\ts_barrier" ::: "memory");
    asm volatile("s_waitcnt vmcnt(0)" : "+v"(snap), "+v"(pf) :: "memory");
    {
        const bool ok = w0 - tc + 128 < 256;
        const float dsum = row_sum(ok ? (float)(int)(w0 - tc) : 0.f), dcnt = row_sum(ok ? 1.f : 0.f);
        if (lane == 16) lds.w[ADJ] = (int)(dsum * __builtin_amdgcn_rcpf(fmaxf(dcnt, 1.f)) * (16.f / (27 - SY - D)));
    }
    const unsigned mine0 = __builtin_amdgcn_readlane(snap, j), mine1 = __builtin_amdgcn_readlane(snap, j + 16);
    const unsigned mine = (lane & 1) ? mine1 : mine0;
    int spin = 0;
    u64 first = 0;
#pragma nounroll
    for (int r = 0; r < R; ++r) {
        while (__builtin_amdgcn_readfirstlane(__hip_atomic_load((lu32*)&lds.w[CNT + r], RLXW)) != NDW) __builtin_amdgcn_s_sleep(1);
        asm volatile("" ::: "memory");
        const unsigned aw = lds.w[ANC + (lane & 7)], a0 = __builtin_amdgcn_readfirstlane(aw);
        const unsigned fin = a0 + (int)(row_sum((float)(int)(aw - a0)) * (1.f / 16));
        v2f p[NDW];
        float m = 0.f;
#pragma unroll
        for (int w = 0; w < NDW; ++w) { p[w] = lds.part[r][w * 64 + lane]; m += p[w].x; }
        m = wave_sum(m) * (1.f / NT);
        float q = 0.f, e = 0.f;
#pragma unroll
        for (int w = 0; w < NDW; ++w) { const float d = p[w].x - m; q += p[w].y; e = fmaf(d, d, e); }
        q = wave_sum(fmaf(e, (float)(CPG * 8), q));
        const u64 body = (lane & 1) ? (u64)fin << 32 : ((u64)__float_as_uint(m) << 32) | (__float_as_uint(q) & ~3u);
        if (lane <= 2 * r)
            __hip_atomic_store(board + (lane & 2 ? 0 : r) * BSTR + 2 * j + (lane & 1),
                               lane & 2 ? first : body | (((mine >> (2 * r)) + 1) & 3u), RLX);
        first = body | ((mine + 2) & 3u);
        u64 val;
        unsigned delay = POLL_FIRST;
#pragma nounroll
        for (;; ++spin) {
            nap(now(), delay);
            delay = POLL_NEXT;
            val = __hip_atomic_load(peer + r * BSTR, RLX);
            if (__all((((unsigned)val ^ (snap >> (2 * r))) & 3u) != 0 || (lane & 16) > 16 * r)) break;
            if (spin >= SPIN_CAP) { val = (lane & 16) ? (u64)fin << 32 : ~0ull; break; }
        }
        const float pm = __uint_as_float((unsigned)(val >> 32)), pq = __uint_as_float((unsigned)val & ~3u);
        const float mu = row_sum(pm) * (1.f / P);
        const float pd = pm - mu;
        const float M2 = row_sum(fmaf(pd * (float)(CPG * 8 * NT), pd, pq));
        const float a = gl * __builtin_amdgcn_rsqf(M2 * (1.f / (CPG * HW)) + 1e-5f);
        if ((lane >> 4) == 2 * r) lds.ab[r][lane & 15] = (v2f){a, bl - mu * a};
        const unsigned slot = fin + (int)(row_sum((float)(int)((unsigned)(val >> 32) - fin)) * (1.f / P));
        asm volatile("" ::: "memory");
        if (lane == 16) __hip_atomic_store((lu32*)&lds.w[RDY + r], r ? slot | 1u : 1u, RLXW);
    }
    asm volatile(".Lgn_end:");
}

extern "C" void kernel_launch(void* const* d_in, const int* in_sizes, int n_in, void* d_out, int out_size, void* d_ws,
                              size_t ws_size, hipStream_t stream) {
    gn_kernel<<<NSET * P, NT + 64, 0, stream>>>(d_in[0], d_out, (const unsigned short*)d_in[1], (const unsigned short*)d_in[2],
                                                (u64*)d_ws);
}
```

```cpp
#include <hip/hip_runtime.h>
#include <stdint.h>

#define LOAD_SPAN 752
#define BEND 24
#define LOAD_LEAD 10
#define ANCHOR_DELAY 30
#define ANCHOR_CAP 120
#define STORE_SPAN 716
#define POLL_FIRST 60
#define POLL_NEXT 30
#define D 5
#define D0 2
#define AW 24
#define SY 8
#define STR2(x) #x
#define STR(x) STR2(x)

typedef float v2f __attribute__((ext_vector_type(2)));
typedef unsigned long long u64;
typedef __attribute__((address_space(1))) u64 gu64;
typedef __attribute__((address_space(3))) unsigned lu32;

constexpr int CPG = 14, HW = 65536;
constexpr int NDW = 8, NT = 512;
constexpr int R = 2, P = 16, NSET = 16;
constexpr int BSTR = 544;
constexpr int SPIN_CAP = 1 << 20;

struct Lds {
    v2f part[R][NT];
    v2f ab[R][16];
    unsigned w[13];
};
constexpr int CNT = 0;
constexpr int RDY = 2;
constexpr int ANC = 4;
constexpr int ADJ = 12;

template <int CTRL>
__device__ __forceinline__ float dpp(float v) {
    return __builtin_bit_cast(float, __builtin_amdgcn_update_dpp(0, __builtin_bit_cast(int, v), CTRL, 0xf, 0xf, true));
}
__device__ __forceinline__ float row_sum(float v) {
    v += dpp<0xB1>(v);
    v += dpp<0x4E>(v);
    v += dpp<0x141>(v);
    v += dpp<0x140>(v);
    return v;
}
__device__ __forceinline__ float wave_sum(float v) {
    const int b = __builtin_bit_cast(int, row_sum(v));
    return (__builtin_bit_cast(float, __builtin_amdgcn_readlane(b, 0)) + __builtin_bit_cast(float, __builtin_amdgcn_readlane(b, 16))) +
           (__builtin_bit_cast(float, __builtin_amdgcn_readlane(b, 32)) + __builtin_bit_cast(float, __builtin_amdgcn_readlane(b, 48)));
}
__device__ __forceinline__ unsigned now() { return (unsigned)__builtin_amdgcn_s_memrealtime(); }
__device__ __forceinline__ void nap(unsigned ticks) {
    const unsigned from = now();
    while (now() - from < ticks) __builtin_amdgcn_s_sleep(1);
}
#define RLX __ATOMIC_RELAXED, __HIP_MEMORY_SCOPE_AGENT
#define RLXW __ATOMIC_RELAXED, __HIP_MEMORY_SCOPE_WORKGROUP

#define V8(a) "v" #a "0", "v" #a "1", "v" #a "2", "v" #a "3", "v" #a "4", "v" #a "5", "v" #a "6", "v" #a "7", "v" #a "8", "v" #a "9"
#define CALL(l) "s_call_b64 s[52:53], .L" l "%=\n"
#define NEXTCH "s_add_u32 s51, s51, s43\n s_cmp_eq_u32 s51, s58\n s_cselect_b32 s51, 0, s51\n"
#define UNPACK(idx)                                                                                                      \
    "s_set_gpr_idx_on " idx ", gpr_idx(SRC1)\n"                                                                          \
    "v_lshlrev_b32 v112, 16, v0\n v_and_b32 v113, s55, v0\n v_lshlrev_b32 v114, 16, v1\n v_and_b32 v115, s55, v1\n"      \
    "v_lshlrev_b32 v116, 16, v2\n v_and_b32 v117, s55, v2\n v_lshlrev_b32 v118, 16, v3\n v_and_b32 v119, s55, v3\n"      \
    "s_set_gpr_idx_off\n"

__global__ __launch_bounds__(NT + 64, 3)
void gn_kernel(const void* __restrict__ x, void* __restrict__ y, const unsigned short* __restrict__ gam,
               const unsigned short* __restrict__ bet, u64* __restrict__ ws) {
    __shared__ Lds lds;
    unsigned t, pf;
    u64 tc;
    const unsigned lbase = (unsigned)(size_t)(__attribute__((address_space(3))) void*)&lds;
    asm volatile(
        "s_load_dwordx2 s[40:41], %[ka], 0\n"
        "s_load_dwordx2 s[44:45], %[ka], 8\n"
        "s_getpc_b64 vcc\n.Lgn_pc:\n v_lshlrev_b32 %[pf], 7, v0\n v_min_u32 %[pf], .Lgn_end-.Lgn_pc, %[pf]\n"
        "global_load_dword %[pf], %[pf], vcc\n"
        "v_readfirstlane_b32 s62, v0\n"
        "s_lshr_b32 s51, %[bid], 4\n"
        "s_mov_b32 s58, 0x1c0000\n"
        "v_mov_b32 %[t], v0\n"
        "s_mul_i32 s54, s51, s58\n"
        "s_and_b32 s49, %[bid], 3\n s_and_b32 s50, %[bid], 12\n s_lshl3_add_u32 s49, s50, s49\n"
        "v_and_b32 v136, 63, v0\n"
        "s_lshl4_add_u32 s49, s49, s62\n s_lshl_b32 s49, s49, 6\n"
        "s_sub_u32 s50, s51, 14\n s_cselect_b32 s51, s51, s50\n s_lshl_b32 s51, s51, 17\n"
        "v_lshl_add_u32 v136, v136, 4, s49\n"
        "s_mov_b32 s43, 0x20000\n s_lshl_b32 s42, s58, 5\n"
        ".set .Lk, 0\n"
        ".rept " STR(D0) "\n"
        "s_add_u32 s[66+.Lk], s54, s51\n" NEXTCH
        ".set .Lk, .Lk+1\n"
        ".endr\n"
        "s_cmpk_lt_u32 s62, 512\n"
        "s_waitcnt lgkmcnt(0)\n"
        "s_memrealtime s[56:57]\n"
        "s_cbranch_scc0 .Lside%=\n"
        ".set .Lk, 0\n"
        ".rept " STR(D0) "\n"
        "buffer_load_dwordx4 v[4*.Lk:4*.Lk+3], v136, s[40:43], s[66+.Lk] offen nt\n"
        ".set .Lk, .Lk+1\n"
        ".endr\n"
        "s_mov_b64 s[46:47], s[42:43]\n"
        "s_mov_b32 s61, s54\n s_lshr_b32 s62, s62, 6\n"
        "s_mov_b32 s48, 0\n s_mov_b32 s55, 0xffff0000\n s_mov_b32 s60, 0\n"
        "v_mov_b32 v133, %[lb]\n v_mov_b32 v132, 1\n v_lshl_add_u32 v134, %[t], 3, %[lb]\n v_mov_b32 v135, %[lb]\n"
        "s_lshl2_add_u32 s63, s62, %[lb]\n v_mov_b32 v125, s63\n"
        "s_add_u32 s59, %[lb], 8192\n"
        "s_movk_i32 s64, %[lp]\n"
        "s_waitcnt lgkmcnt(0)\n"
        "s_lshl_b32 s56, s56, 4\n"
        "s_sub_u32 s50, %[l0], s56\n"
        ".rept " STR(D) "-" STR(D0) "\n"
        CALL("pace")
        "buffer_load_dwordx4 v[4*.Lk:4*.Lk+3], v136, s[40:43], s49 offen nt\n"
        ".set .Lk, .Lk+1\n"
        ".endr\n"
        ".set .Li, 0\n .set .Ls, 0\n"
        ".rept 28\n"
        "s_waitcnt vmcnt(.Lk-1-.Li+.Ls)\n"
        ".if .Li == 1\n s_barrier\n .endif\n"
        ".if .Li == " STR(SY) "\n ds_read_b32 v112, v135 offset:8496\n s_waitcnt lgkmcnt(0)\n v_readfirstlane_b32 s56, v112\n s_add_u32 s64, s64, s56\n .endif\n"
        ".if .Li == 0 || .Li == 14\n"
        "v_lshlrev_b32 v124, 16, v[4*.Li]\n v_mov_b64 v[126:127], 0\n v_mov_b64 v[128:129], 0\n"
        ".endif\n"
        CALL("stats")
        ".if .Li == 13 || .Li == 27\n" CALL("part") ".endif\n"
        ".if .Lk < 28\n"
        ".if .Lk == 14\n s_add_u32 s54, s54, 0x1c00000\n s_addk_i32 s64, " STR(BEND) "*16/28\n .endif\n"
        CALL("pace")
        "buffer_load_dwordx4 v[4*.Lk:4*.Lk+3], v136, s[40:43], s49 offen nt\n"
        ".set .Lk, .Lk+1\n"
        ".if .Lk == 28\n s_mov_b32 s54, s61\n" CALL("ready") ".endif\n"
        ".elseif .Li >= " STR(AW) "-1 && .Li < 27\n"
        CALL("store")
        ".set .Ls, .Ls+1\n"
        ".endif\n"
        ".set .Li, .Li+1\n"
        ".endr\n"
        ".La%=:\n" CALL("store") "s_cmp_lg_u32 s60, 56\n s_cbranch_scc1 .La%=\n"
        "s_add_u32 s54, s54, 0x1c00000\n s_add_u32 s59, s59, 128\n"
        CALL("ready")
        "s_sub_u32 s50, s56, 14*%[sp]\n"
        ".Lb%=:\n" CALL("store") "s_cmpk_lg_u32 s60, 112\n s_cbranch_scc1 .Lb%=\n"
        "s_endpgm\n"

        ".Lstats%=:\n"
        UNPACK("s48")
        "v_pk_add_f32 v[112:113], v[112:113], v[124:125] op_sel_hi:[1,0] neg_lo:[0,1] neg_hi:[0,1]\n"
        "v_pk_add_f32 v[114:115], v[114:115], v[124:125] op_sel_hi:[1,0] neg_lo:[0,1] neg_hi:[0,1]\n"
        "v_pk_add_f32 v[116:117], v[116:117], v[124:125] op_sel_hi:[1,0] neg_lo:[0,1] neg_hi:[0,1]\n"
        "v_pk_add_f32 v[118:119], v[118:119], v[124:125] op_sel_hi:[1,0] neg_lo:[0,1] neg_hi:[0,1]\n"
        "s_add_u32 s48, s48, 4\n"
        "v_pk_add_f32 v[126:127], v[126:127], v[112:113]\n v_pk_fma_f32 v[128:129], v[112:113], v[112:113], v[128:129]\n"
        "v_pk_add_f32 v[126:127], v[126:127], v[114:115]\n v_pk_fma_f32 v[128:129], v[114:115], v[114:115], v[128:129]\n"
        "v_pk_add_f32 v[126:127], v[126:127], v[116:117]\n v_pk_fma_f32 v[128:129], v[116:117], v[116:117], v[128:129]\n"
        "v_pk_add_f32 v[126:127], v[126:127], v[118:119]\n v_pk_fma_f32 v[128:129], v[118:119], v[118:119], v[128:129]\n"
        "s_setpc_b64 s[52:53]\n"

        ".Lpart%=:\n"
        "v_add_f32 v126, v126, v127\n v_add_f32 v128, v128, v129\n"
        "v_mul_f32 v127, 0x3c124925, v126\n"
        "v_add_f32 v112, v124, v127\n v_fma_f32 v113, -v126, v127, v128\n"
        "ds_write_b64 v134, v[112:113]\n"
        "s_mov_b64 exec, 1\n ds_add_u32 v133, v132 offset:8448\n s_mov_b64 exec, -1\n"
        "v_add_u32 v134, 0x1000, v134\n v_add_u32 v133, 4, v133\n"
        "s_setpc_b64 s[52:53]\n"

        ".Lp1%=:\n s_sleep 1\n"
        ".Lpace%=:\n s_memrealtime s[56:57]\n s_add_u32 s49, s54, s51\n s_waitcnt lgkmcnt(0)\n"
        "s_lshl4_add_u32 s56, s56, s50\n s_cmp_lt_i32 s56, 0\n s_cbranch_scc1 .Lp1%=\n"
        "s_sub_u32 s50, s50, s64\n"
        NEXTCH
        "s_setpc_b64 s[52:53]\n"

        ".Lr1%=:\n s_sleep 1\n"
        ".Lready%=:\n ds_read_b32 v112, v135 offset:8456\n s_waitcnt lgkmcnt(0)\n v_readfirstlane_b32 s56, v112\n"
        "s_cmp_eq_u32 s56, 0\n s_cbranch_scc1 .Lr1%=\n"
        "v_add_u32 v135, 4, v135\n"
        "s_setpc_b64 s[52:53]\n"

        ".Lstore%=:\n"
        "s_lshr_b32 s49, s51, 14\n s_add_u32 s49, s49, s59\n v_mov_b32 v130, s49\n ds_read_b64 v[130:131], v130\n"
        UNPACK("s60")
        "s_add_u32 s60, s60, 4\n s_add_u32 s49, s54, s51\n"
        "s_waitcnt lgkmcnt(0)\n"
        "s_memrealtime s[56:57]\n"
        "v_pk_fma_f32 v[112:113], v[112:113], v[130:131], v[130:131] op_sel:[0,0,1] op_sel_hi:[1,0,1]\n"
        "v_pk_fma_f32 v[114:115], v[114:115], v[130:131], v[130:131] op_sel:[0,0,1] op_sel_hi:[1,0,1]\n"
        "v_pk_fma_f32 v[116:117], v[116:117], v[130:131], v[130:131] op_sel:[0,0,1] op_sel_hi:[1,0,1]\n"
        "v_pk_fma_f32 v[118:119], v[118:119], v[130:131], v[130:131] op_sel:[0,0,1] op_sel_hi:[1,0,1]\n"
        "s_cmp_eq_u32 s60, 4\n"
        "v_cvt_pk_bf16_f32 v120, v112, v113\n v_cvt_pk_bf16_f32 v121, v114, v115\n"
        "v_cvt_pk_bf16_f32 v122, v116, v117\n v_cvt_pk_bf16_f32 v123, v118, v119\n"
        "s_cbranch_scc1 .Lanchor%=\n"
        ".Ls2%=:\n s_waitcnt lgkmcnt(0)\n s_lshl4_add_u32 s56, s56, s50\n s_cmp_lt_i32 s56, 0\n s_cbranch_scc1 .Ls1%=\n"
        "buffer_store_dwordx4 v[120:123], v136, s[44:47], s49 offen sc1\n"
        "s_addk_i32 s50, -%[sp]\n"
        NEXTCH
        "s_setpc_b64 s[52:53]\n"
        ".Lanchor%=:\n s_waitcnt vmcnt(27-" STR(AW) ") lgkmcnt(0)\n s_memrealtime s[56:57]\n s_waitcnt lgkmcnt(0)\n"
        "s_lshl4_add_u32 s56, s56, s50\n s_min_i32 s56, s56, 16*" STR(ANCHOR_CAP) "\n s_addk_i32 s56, 16*" STR(ANCHOR_DELAY) "\n s_sub_u32 s50, s50, s56\n"
        "v_mov_b32 v112, s50\n ds_write_b32 v125, v112 offset:8464\n"
        ".Ls1%=:\n s_sleep 1\n s_memrealtime s[56:57]\n s_branch .Ls2%=\n"

        ".Lside%=:\n"
        "s_waitcnt lgkmcnt(0)\n s_mov_b64 %[tc], s[56:57]\n"
        : [t] "=&v"(t), [pf] "=&v"(pf), [tc] "=&s"(tc)
        : [ka] "s"(__builtin_amdgcn_kernarg_segment_ptr()), [bid] "s"(blockIdx.x), [lb] "s"(lbase), "{v0}"(threadIdx.x),
          [lp] "i"(LOAD_SPAN * 16 / 28), [l0] "i"(LOAD_LEAD * 16 - D0 * (LOAD_SPAN * 16 / 28)), [sp] "i"(STORE_SPAN * 16 / 28)
        : "memory", "scc", "vcc", "v1", "v2", "v3", "v4", "v5", "v6", "v7", "v8", "v9", V8(1), V8(2), V8(3), V8(4), V8(5), V8(6), V8(7),
          V8(8), V8(9), V8(10), V8(11), V8(12), "v130", "v131", "v132", "v133", "v134", "v135", "v136",
          "s40", "s41", "s42", "s43", "s44", "s45", "s46", "s47", "s48", "s49", "s50", "s51", "s52", "s53", "s54", "s55", "s56", "s57",
          "s58", "s59", "s60", "s61", "s62", "s63", "s64", "s66", "s67");

    __builtin_amdgcn_s_setprio(3);
    const int lane = t & 63;
    const unsigned set = blockIdx.x / P, j = blockIdx.x % P;
    if (lane < 13) lds.w[lane] = 0;
    gu64* board = (gu64*)ws + (size_t)set * (R * BSTR);
    if (lane == 0) __hip_atomic_store(board + 2 * j + 1, tc, RLX);
    const int cl = (set + (lane >> 5) * NSET) * CPG + min(lane & 15, CPG - 1);
    const float gl = __uint_as_float((unsigned)gam[cl] << 16), bl = __uint_as_float((unsigned)bet[cl] << 16);
    asm volatile("s_waitcnt lgkmcnt(0)\n\ts_barrier" ::: "memory");
    gu64* peer = board + (lane & (P - 1)) * 2 + ((lane >> 4) & 1);
    const u64 w0 = __hip_atomic_load(peer, RLX);
    unsigned snap = ((unsigned)w0 & 3u) | ((unsigned)__hip_atomic_load(peer + BSTR, RLX) & 3u) << 2;
    asm volatile("s_waitcnt vmcnt(0)" : "+v"(snap), "+v"(pf) :: "memory");
    {
        const bool ok = w0 - tc + 128 < 256;
        const float dsum = row_sum(ok ? (float)(int)(w0 - tc) : 0.f), dcnt = row_sum(ok ? 1.f : 0.f);
        if (lane == 16) lds.w[ADJ] = (int)(dsum * __builtin_amdgcn_rcpf(fmaxf(dcnt, 1.f)) * (16.f / (27 - SY - D)));
    }
    const unsigned mine0 = __builtin_amdgcn_readlane(snap, j), mine1 = __builtin_amdgcn_readlane(snap, j + 16);
    const unsigned mine = (lane & 1) ? mine1 : mine0;
    int spin = 0;
    u64 first = 0;
#pragma nounroll
    for (int r = 0; r < R; ++r) {
        while (__builtin_amdgcn_readfirstlane(__hip_atomic_load((lu32*)&lds.w[CNT + r], RLXW)) != NDW) __builtin_amdgcn_s_sleep(1);
        asm volatile("" ::: "memory");
        const unsigned aw = lds.w[ANC + (lane & 7)], a0 = __builtin_amdgcn_readfirstlane(aw);
        const unsigned fin = a0 + (int)(row_sum((float)(int)(aw - a0)) * (1.f / 16));
        v2f p[NDW];
        float m = 0.f;
#pragma unroll
        for (int w = 0; w < NDW; ++w) { p[w] = lds.part[r][w * 64 + lane]; m += p[w].x; }
        m = wave_sum(m) * (1.f / NT);
        float q = 0.f, e = 0.f;
#pragma unroll
        for (int w = 0; w < NDW; ++w) { const float d = p[w].x - m; q += p[w].y; e = fmaf(d, d, e); }
        q = wave_sum(fmaf(e, (float)(CPG * 8), q));
        const u64 body = (lane & 1) ? (u64)fin << 32 : ((u64)__float_as_uint(m) << 32) | (__float_as_uint(q) & ~3u);
        if (lane <= 2 * r)
            __hip_atomic_store(board + (lane & 2 ? 0 : r) * BSTR + 2 * j + (lane & 1),
                               lane & 2 ? first : body | (((mine >> (2 * r)) + 1) & 3u), RLX);
        first = body | ((mine + 2) & 3u);
        u64 val;
        unsigned delay = POLL_FIRST;
#pragma nounroll
        for (;; ++spin) {
            nap(delay);
            delay = POLL_NEXT;
            val = __hip_atomic_load(peer + r * BSTR, RLX);
            if (__all((((unsigned)val ^ (snap >> (2 * r))) & 3u) != 0 || (lane & 16) > 16 * r)) break;
            if (spin >= SPIN_CAP) { val = (lane & 16) ? (u64)fin << 32 : ~0ull; break; }
        }
        const float pm = __uint_as_float((unsigned)(val >> 32)), pq = __uint_as_float((unsigned)val & ~3u);
        const float mu = row_sum(pm) * (1.f / P);
        const float pd = pm - mu;
        const float M2 = row_sum(fmaf(pd * (float)(CPG * 8 * NT), pd, pq));
        const float a = gl * __builtin_amdgcn_rsqf(M2 * (1.f / (CPG * HW)) + 1e-5f);
        if ((lane >> 4) == 2 * r) lds.ab[r][lane & 15] = (v2f){a, bl - mu * a};
        const unsigned slot = fin + (int)(row_sum((float)(int)((unsigned)(val >> 32) - fin)) * (1.f / P));
        asm volatile("" ::: "memory");
        if (lane == 16) __hip_atomic_store((lu32*)&lds.w[RDY + r], r ? slot | 1u : 1u, RLXW);
    }
    asm volatile(".Lgn_end:");
}

extern "C" void kernel_launch(void* const* d_in, const int* in_sizes, int n_in, void* d_out, int out_size, void* d_ws,
                              size_t ws_size, hipStream_t stream) {
    gn_kernel<<<NSET * P, NT + 64, 0, stream>>>(d_in[0], d_out, (const unsigned short*)d_in[1], (const unsigned short*)d_in[2],
                                                (u64*)d_ws);
}
```

```cpp
#include <hip/hip_runtime.h>
#include <stdint.h>

#define LOAD_SPAN 752
#define BEND 24
#define LOAD_LEAD 10
#define ANCHOR_DELAY 30
#define ANCHOR_CAP 120
#define STORE_SPAN 716
#define POLL_FIRST 60
#define POLL_NEXT 30
#define D 5
#define D0 2
#define AW 24
#define SY 8
#define STR2(x) #x
#define STR(x) STR2(x)

typedef float v2f __attribute__((ext_vector_type(2)));
typedef unsigned v8u __attribute__((ext_vector_type(8)));
typedef unsigned long long u64;
typedef __attribute__((address_space(1))) u64 gu64;
typedef __attribute__((address_space(3))) unsigned lu32;

constexpr int CPG = 14, HW = 65536;
constexpr int NDW = 8, NT = 512;
constexpr int R = 2, P = 16, NSET = 16;
constexpr int BSTR = 544;
constexpr int SPIN_CAP = 1 << 20;

struct Lds {
    v2f part[R][NT];
    v2f ab[R][16];
    unsigned w[13];
};
constexpr int CNT = 0;
constexpr int RDY = 2;
constexpr int ANC = 4;
constexpr int ADJ = 12;

template <int CTRL>
__device__ __forceinline__ float dpp(float v) {
    return __builtin_bit_cast(float, __builtin_amdgcn_update_dpp(0, __builtin_bit_cast(int, v), CTRL, 0xf, 0xf, true));
}
__device__ __forceinline__ float row_sum(float v) {
    v += dpp<0xB1>(v);
    v += dpp<0x4E>(v);
    v += dpp<0x141>(v);
    v += dpp<0x140>(v);
    return v;
}
__device__ __forceinline__ float wave_sum(float v) {
    const int b = __builtin_bit_cast(int, row_sum(v));
    return (__builtin_bit_cast(float, __builtin_amdgcn_readlane(b, 0)) + __builtin_bit_cast(float, __builtin_amdgcn_readlane(b, 16))) +
           (__builtin_bit_cast(float, __builtin_amdgcn_readlane(b, 32)) + __builtin_bit_cast(float, __builtin_amdgcn_readlane(b, 48)));
}
__device__ __forceinline__ unsigned now() { return (unsigned)__builtin_amdgcn_s_memrealtime(); }
__device__ __forceinline__ void nap(unsigned ticks) {
    const unsigned from = now();
    while (now() - from < ticks) __builtin_amdgcn_s_sleep(1);
}
#define RLX __ATOMIC_RELAXED, __HIP_MEMORY_SCOPE_AGENT
#define RLXW __ATOMIC_RELAXED, __HIP_MEMORY_SCOPE_WORKGROUP

#define V8(a) "v" #a "0", "v" #a "1", "v" #a "2", "v" #a "3", "v" #a "4", "v" #a "5", "v" #a "6", "v" #a "7", "v" #a "8", "v" #a "9"
#define CALL(l) "s_call_b64 s[52:53], .L" l "%=\n"
#define NEXTCH "s_add_u32 s51, s51, s43\n s_cmp_eq_u32 s51, s58\n s_cselect_b32 s51, 0, s51\n"
#define UNPACK(idx)                                                                                                      \
    "s_set_gpr_idx_on " idx ", gpr_idx(SRC1)\n"                                                                          \
    "v_lshlrev_b32 v112, 16, v0\n v_and_b32 v113, s55, v0\n v_lshlrev_b32 v114, 16, v1\n v_and_b32 v115, s55, v1\n"      \
    "v_lshlrev_b32 v116, 16, v2\n v_and_b32 v117, s55, v2\n v_lshlrev_b32 v118, 16, v3\n v_and_b32 v119, s55, v3\n"      \
    "s_set_gpr_idx_off\n"

__global__ __launch_bounds__(NT + 64, 3) __attribute__((aligned(4096)))
void gn_kernel(const void* __restrict__ x, void* __restrict__ y, const unsigned short* __restrict__ gam,
               const unsigned short* __restrict__ bet, u64* __restrict__ ws) {
    __shared__ Lds lds;
    unsigned t, pf;
    u64 tc;
    const unsigned lbase = (unsigned)(size_t)(__attribute__((address_space(3))) void*)&lds;
    asm volatile(
        "s_load_dwordx2 s[40:41], %[ka], 0\n"
        "s_load_dwordx2 s[44:45], %[ka], 8\n"
        "s_getpc_b64 vcc\n.Lgn_pc:\n v_lshlrev_b32 %[pf], 7, v0\n v_min_u32 %[pf], .Lgn_end-.Lgn_pc, %[pf]\n"
        "global_load_dword %[pf], %[pf], vcc\n"
        "v_readfirstlane_b32 s62, v0\n"
        "s_lshr_b32 s51, %[bid], 4\n"
        "s_mov_b32 s58, 0x1c0000\n"
        "v_mov_b32 %[t], v0\n"
        "s_mul_i32 s54, s51, s58\n"
        "s_and_b32 s49, %[bid], 3\n s_and_b32 s50, %[bid], 12\n s_lshl3_add_u32 s49, s50, s49\n"
        "v_and_b32 v136, 63, v0\n"
        "s_lshl4_add_u32 s49, s49, s62\n s_lshl_b32 s49, s49, 6\n"
        "s_sub_u32 s50, s51, 14\n s_cselect_b32 s51, s51, s50\n s_lshl_b32 s51, s51, 17\n"
        "v_lshl_add_u32 v136, v136, 4, s49\n"
        "s_mov_b32 s43, 0x20000\n s_lshl_b32 s42, s58, 5\n"
        ".set .Lk, 0\n"
        ".rept " STR(D0) "\n"
        "s_add_u32 s[66+.Lk], s54, s51\n" NEXTCH
        ".set .Lk, .Lk+1\n"
        ".endr\n"
        "s_cmpk_lt_u32 s62, 512\n"
        "s_waitcnt lgkmcnt(0)\n"
        "s_memrealtime s[56:57]\n"
        "s_cbranch_scc0 .Lside%=\n"
        ".set .Lk, 0\n"
        ".rept " STR(D0) "\n"
        "buffer_load_dwordx4 v[4*.Lk:4*.Lk+3], v136, s[40:43], s[66+.Lk] offen nt\n"
        ".set .Lk, .Lk+1\n"
        ".endr\n"
        "s_mov_b64 s[46:47], s[42:43]\n"
        "s_mov_b32 s61, s54\n s_lshr_b32 s62, s62, 6\n"
        "s_mov_b32 s48, 0\n s_mov_b32 s55, 0xffff0000\n s_mov_b32 s60, 0\n"
        "v_mov_b32 v133, %[lb]\n v_mov_b32 v132, 1\n v_lshl_add_u32 v134, %[t], 3, %[lb]\n v_mov_b32 v135, %[lb]\n"
        "s_lshl2_add_u32 s63, s62, %[lb]\n v_mov_b32 v125, s63\n"
        "s_add_u32 s59, %[lb], 8192\n"
        "s_movk_i32 s64, %[lp]\n"
        "s_waitcnt lgkmcnt(0)\n"
        "s_lshl_b32 s56, s56, 4\n"
        "s_sub_u32 s50, %[l0], s56\n"
        ".rept " STR(D) "-" STR(D0) "\n"
        CALL("pace")
        "buffer_load_dwordx4 v[4*.Lk:4*.Lk+3], v136, s[40:43], s49 offen nt\n"
        ".set .Lk, .Lk+1\n"
        ".endr\n"
        ".set .Li, 0\n .set .Ls, 0\n"
        ".rept 28\n"
        "s_waitcnt vmcnt(.Lk-1-.Li+.Ls)\n"
        ".if .Li == 1\n s_barrier\n .endif\n"
        ".if .Li == " STR(SY) "\n ds_read_b32 v112, v135 offset:8496\n s_waitcnt lgkmcnt(0)\n v_readfirstlane_b32 s56, v112\n s_add_u32 s64, s64, s56\n .endif\n"
        ".if .Li == 0 || .Li == 14\n"
        "v_lshlrev_b32 v124, 16, v[4*.Li]\n v_mov_b64 v[126:127], 0\n v_mov_b64 v[128:129], 0\n"
        ".endif\n"
        CALL("stats")
        ".if .Li == 13 || .Li == 27\n" CALL("part") ".endif\n"
        ".if .Lk < 28\n"
        ".if .Lk == 14\n s_add_u32 s54, s54, 0x1c00000\n s_addk_i32 s64, " STR(BEND) "*16/28\n .endif\n"
        CALL("pace")
        "buffer_load_dwordx4 v[4*.Lk:4*.Lk+3], v136, s[40:43], s49 offen nt\n"
        ".set .Lk, .Lk+1\n"
        ".if .Lk == 28\n s_mov_b32 s54, s61\n" CALL("ready") ".endif\n"
        ".elseif .Li >= " STR(AW) "-1 && .Li < 27\n"
        CALL("store")
        ".set .Ls, .Ls+1\n"
        ".endif\n"
        ".set .Li, .Li+1\n"
        ".endr\n"
        ".La%=:\n" CALL("store") "s_cmp_lg_u32 s60, 56\n s_cbranch_scc1 .La%=\n"
        "s_add_u32 s54, s54, 0x1c00000\n s_add_u32 s59, s59, 128\n"
        CALL("ready")
        "s_sub_u32 s50, s56, 14*%[sp]\n"
        ".Lb%=:\n" CALL("store") "s_cmpk_lg_u32 s60, 112\n s_cbranch_scc1 .Lb%=\n"
        "s_endpgm\n"

        ".Lstats%=:\n"
        UNPACK("s48")
        "v_pk_add_f32 v[112:113], v[112:113], v[124:125] op_sel_hi:[1,0] neg_lo:[0,1] neg_hi:[0,1]\n"
        "v_pk_add_f32 v[114:115], v[114:115], v[124:125] op_sel_hi:[1,0] neg_lo:[0,1] neg_hi:[0,1]\n"
        "v_pk_add_f32 v[116:117], v[116:117], v[124:125] op_sel_hi:[1,0] neg_lo:[0,1] neg_hi:[0,1]\n"
        "v_pk_add_f32 v[118:119], v[118:119], v[124:125] op_sel_hi:[1,0] neg_lo:[0,1] neg_hi:[0,1]\n"
        "s_add_u32 s48, s48, 4\n"
        "v_pk_add_f32 v[126:127], v[126:127], v[112:113]\n v_pk_fma_f32 v[128:129], v[112:113], v[112:113], v[128:129]\n"
        "v_pk_add_f32 v[126:127], v[126:127], v[114:115]\n v_pk_fma_f32 v[128:129], v[114:115], v[114:115], v[128:129]\n"
        "v_pk_add_f32 v[126:127], v[126:127], v[116:117]\n v_pk_fma_f32 v[128:129], v[116:117], v[116:117], v[128:129]\n"
        "v_pk_add_f32 v[126:127], v[126:127], v[118:119]\n v_pk_fma_f32 v[128:129], v[118:119], v[118:119], v[128:129]\n"
        "s_setpc_b64 s[52:53]\n"

        ".Lpart%=:\n"
        "v_add_f32 v126, v126, v127\n v_add_f32 v128, v128, v129\n"
        "v_mul_f32 v127, 0x3c124925, v126\n"
        "v_add_f32 v112, v124, v127\n v_fma_f32 v113, -v126, v127, v128\n"
        "ds_write_b64 v134, v[112:113]\n"
        "s_mov_b64 exec, 1\n ds_add_u32 v133, v132 offset:8448\n s_mov_b64 exec, -1\n"
        "v_add_u32 v134, 0x1000, v134\n v_add_u32 v133, 4, v133\n"
        "s_setpc_b64 s[52:53]\n"

        ".Lp1%=:\n s_sleep 1\n"
        ".Lpace%=:\n s_memrealtime s[56:57]\n s_add_u32 s49, s54, s51\n s_waitcnt lgkmcnt(0)\n"
        "s_lshl4_add_u32 s56, s56, s50\n s_cmp_lt_i32 s56, 0\n s_cbranch_scc1 .Lp1%=\n"
        "s_sub_u32 s50, s50, s64\n"
        NEXTCH
        "s_setpc_b64 s[52:53]\n"

        ".Lr1%=:\n s_sleep 1\n"
        ".Lready%=:\n ds_read_b32 v112, v135 offset:8456\n s_waitcnt lgkmcnt(0)\n v_readfirstlane_b32 s56, v112\n"
        "s_cmp_eq_u32 s56, 0\n s_cbranch_scc1 .Lr1%=\n"
        "v_add_u32 v135, 4, v135\n"
        "s_setpc_b64 s[52:53]\n"

        ".Lstore%=:\n"
        "s_lshr_b32 s49, s51, 14\n s_add_u32 s49, s49, s59\n v_mov_b32 v130, s49\n ds_read_b64 v[130:131], v130\n"
        UNPACK("s60")
        "s_add_u32 s60, s60, 4\n s_add_u32 s49, s54, s51\n"
        "s_waitcnt lgkmcnt(0)\n"
        "s_memrealtime s[56:57]\n"
        "v_pk_fma_f32 v[112:113], v[112:113], v[130:131], v[130:131] op_sel:[0,0,1] op_sel_hi:[1,0,1]\n"
        "v_pk_fma_f32 v[114:115], v[114:115], v[130:131], v[130:131] op_sel:[0,0,1] op_sel_hi:[1,0,1]\n"
        "v_pk_fma_f32 v[116:117], v[116:117], v[130:131], v[130:131] op_sel:[0,0,1] op_sel_hi:[1,0,1]\n"
        "v_pk_fma_f32 v[118:119], v[118:119], v[130:131], v[130:131] op_sel:[0,0,1] op_sel_hi:[1,0,1]\n"
        "s_cmp_eq_u32 s60, 4\n"
        "v_cvt_pk_bf16_f32 v120, v112, v113\n v_cvt_pk_bf16_f32 v121, v114, v115\n"
        "v_cvt_pk_bf16_f32 v122, v116, v117\n v_cvt_pk_bf16_f32 v123, v118, v119\n"
        "s_cbranch_scc1 .Lanchor%=\n"
        ".Ls2%=:\n s_waitcnt lgkmcnt(0)\n s_lshl4_add_u32 s56, s56, s50\n s_cmp_lt_i32 s56, 0\n s_cbranch_scc1 .Ls1%=\n"
        "buffer_store_dwordx4 v[120:123], v136, s[44:47], s49 offen sc1\n"
        "s_addk_i32 s50, -%[sp]\n"
        NEXTCH
        "s_setpc_b64 s[52:53]\n"
        ".Lanchor%=:\n s_waitcnt vmcnt(27-" STR(AW) ") lgkmcnt(0)\n s_memrealtime s[56:57]\n s_waitcnt lgkmcnt(0)\n"
        "s_lshl4_add_u32 s56, s56, s50\n s_min_i32 s56, s56, 16*" STR(ANCHOR_CAP) "\n s_addk_i32 s56, 16*" STR(ANCHOR_DELAY) "\n s_sub_u32 s50, s50, s56\n"
        "v_mov_b32 v112, s50\n ds_write_b32 v125, v112 offset:8464\n"
        ".Ls1%=:\n s_sleep 1\n s_memrealtime s[56:57]\n s_branch .Ls2%=\n"

        ".Lside%=:\n"
        "s_waitcnt lgkmcnt(0)\n s_mov_b64 %[tc], s[56:57]\n"
        : [t] "=&v"(t), [pf] "=&v"(pf), [tc] "=&s"(tc)
        : [ka] "s"(__builtin_amdgcn_kernarg_segment_ptr()), [bid] "s"(blockIdx.x), [lb] "s"(lbase), "{v0}"(threadIdx.x),
          [lp] "i"(LOAD_SPAN * 16 / 28), [l0] "i"(LOAD_LEAD * 16 - D0 * (LOAD_SPAN * 16 / 28)), [sp] "i"(STORE_SPAN * 16 / 28)
        : "memory", "scc", "vcc", "v1", "v2", "v3", "v4", "v5", "v6", "v7", "v8", "v9", V8(1), V8(2), V8(3), V8(4), V8(5), V8(6), V8(7),
          V8(8), V8(9), V8(10), V8(11), V8(12), "v130", "v131", "v132", "v133", "v134", "v135", "v136",
          "s40", "s41", "s42", "s43", "s44", "s45", "s46", "s47", "s48", "s49", "s50", "s51", "s52", "s53", "s54", "s55", "s56", "s57",
          "s58", "s59", "s60", "s61", "s62", "s63", "s64", "s66", "s67");

    __builtin_amdgcn_s_setprio(3);
    const int lane = t & 63;
    const unsigned set = blockIdx.x / P, j = blockIdx.x % P;
    if (lane < 13) lds.w[lane] = 0;
    gu64* board = (gu64*)ws + (size_t)set * (R * BSTR);
    if (lane == 0) __hip_atomic_store(board + 2 * j + 1, tc, RLX);
    v8u g0, b0, g1, b1;
    asm volatile("s_waitcnt lgkmcnt(0)\n s_load_dwordx8 %0, %2, %4\n s_load_dwordx8 %1, %3, %4\n s_barrier\n s_waitcnt lgkmcnt(0)"
                 : "=&s"(g0), "=&s"(b0) : "s"(gam), "s"(bet), "s"(set * (CPG * 2)) : "memory");
    gu64* peer = board + (lane & (P - 1)) * 2 + ((lane >> 4) & 1);
    const u64 w0 = __hip_atomic_load(peer, RLX);
    unsigned snap = ((unsigned)w0 & 3u) | ((unsigned)__hip_atomic_load(peer + BSTR, RLX) & 3u) << 2;
    asm volatile("s_waitcnt vmcnt(0)" : "+v"(snap), "+v"(pf) :: "memory");
    {
        const bool ok = w0 - tc + 128 < 256;
        const float dsum = row_sum(ok ? (float)(int)(w0 - tc) : 0.f), dcnt = row_sum(ok ? 1.f : 0.f);
        if (lane == 16) lds.w[ADJ] = (int)(dsum * __builtin_amdgcn_rcpf(fmaxf(dcnt, 1.f)) * (16.f / (27 - SY - D)));
    }
    asm volatile("s_load_dwordx8 %0, %2, %4\n s_load_dwordx8 %1, %3, %4\n s_waitcnt lgkmcnt(0)"
                 : "=&s"(g1), "=&s"(b1) : "s"(gam), "s"(bet), "s"((set + NSET) * (CPG * 2) - 4) : "memory");
    int gw = 0, bw = 0;
#pragma unroll
    for (int k = 0; k < CPG / 2; ++k)
        asm("v_writelane_b32 %0, %2, %6\n v_writelane_b32 %1, %3, %6\n v_writelane_b32 %0, %4, %7\n v_writelane_b32 %1, %5, %7"
            : "+v"(gw), "+v"(bw) : "s"(g0[k]), "s"(b0[k]), "s"(g1[k + 1]), "s"(b1[k + 1]), "i"(2 * k), "i"(32 + 2 * k));
    asm("s_nop 1" : "+v"(gw), "+v"(bw));
    gw = __builtin_amdgcn_update_dpp(0, gw, 0xA0, 0xf, 0xf, true);
    bw = __builtin_amdgcn_update_dpp(0, bw, 0xA0, 0xf, 0xf, true);
    const float gl = __uint_as_float(lane & 1 ? gw & 0xffff0000u : (unsigned)gw << 16);
    const float bl = __uint_as_float(lane & 1 ? bw & 0xffff0000u : (unsigned)bw << 16);
    const unsigned mine0 = __builtin_amdgcn_readlane(snap, j), mine1 = __builtin_amdgcn_readlane(snap, j + 16);
    const unsigned mine = (lane & 1) ? mine1 : mine0;
    int spin = 0;
    u64 first = 0;
#pragma nounroll
    for (int r = 0; r < R; ++r) {
        while (__builtin_amdgcn_readfirstlane(__hip_atomic_load((lu32*)&lds.w[CNT + r], RLXW)) != NDW) __builtin_amdgcn_s_sleep(1);
        asm volatile("" ::: "memory");
        const unsigned aw = lds.w[ANC + (lane & 7)], a0 = __builtin_amdgcn_readfirstlane(aw);
        const unsigned fin = a0 + (int)(row_sum((float)(int)(aw - a0)) * (1.f / 16));
        v2f p[NDW];
        float m = 0.f;
#pragma unroll
        for (int w = 0; w < NDW; ++w) { p[w] = lds.part[r][w * 64 + lane]; m += p[w].x; }
        m = wave_sum(m) * (1.f / NT);
        float q = 0.f, e = 0.f;
#pragma unroll
        for (int w = 0; w < NDW; ++w) { const float d = p[w].x - m; q += p[w].y; e = fmaf(d, d, e); }
        q = wave_sum(fmaf(e, (float)(CPG * 8), q));
        const u64 body = (lane & 1) ? (u64)fin << 32 : ((u64)__float_as_uint(m) << 32) | (__float_as_uint(q) & ~3u);
        if (lane <= 2 * r)
            __hip_atomic_store(board + (lane & 2 ? 0 : r) * BSTR + 2 * j + (lane & 1),
                               lane & 2 ? first : body | (((mine >> (2 * r)) + 1) & 3u), RLX);
        first = body | ((mine + 2) & 3u);
        u64 val;
        unsigned delay = POLL_FIRST;
#pragma nounroll
        for (;; ++spin) {
            nap(delay);
            delay = POLL_NEXT;
            val = __hip_atomic_load(peer + r * BSTR, RLX);
            if (__all((((unsigned)val ^ (snap >> (2 * r))) & 3u) != 0 || (lane & 16) > 16 * r)) break;
            if (spin >= SPIN_CAP) { val = (lane & 16) ? (u64)fin << 32 : ~0ull; break; }
        }
        const float pm = __uint_as_float((unsigned)(val >> 32)), pq = __uint_as_float((unsigned)val & ~3u);
        const float mu = row_sum(pm) * (1.f / P);
        const float pd = pm - mu;
        const float M2 = row_sum(fmaf(pd * (float)(CPG * 8 * NT), pd, pq));
        const float a = gl * __builtin_amdgcn_rsqf(M2 * (1.f / (CPG * HW)) + 1e-5f);
        if ((lane >> 4) == 2 * r) lds.ab[r][lane & 15] = (v2f){a, bl - mu * a};
        const unsigned slot = fin + (int)(row_sum((float)(int)((unsigned)(val >> 32) - fin)) * (1.f / P));
        asm volatile("" ::: "memory");
        if (lane == 16) __hip_atomic_store((lu32*)&lds.w[RDY + r], r ? slot | 1u : 1u, RLXW);
    }
    asm volatile(".Lgn_end:");
}

extern "C" void kernel_launch(void* const* d_in, const int* in_sizes, int n_in, void* d_out, int out_size, void* d_ws,
                              size_t ws_size, hipStream_t stream) {
    gn_kernel<<<NSET * P, NT + 64, 0, stream>>>(d_in[0], d_out, (const unsigned short*)d_in[1], (const unsigned short*)d_in[2],
                                                (u64*)d_ws);
}
```

```cpp
#include <hip/hip_runtime.h>
#include <stdint.h>

#define LOAD_SPAN 743
#define BEND 72
#define LOAD_LEAD 10
#define ANCHOR_DELAY 4
#define ANCHOR_CAP 120
#define STORE_SPAN 719
#define POLL_0 100
#define POLL_1 50
#define POLL_NEXT 20
#define D 5
#define D0 2
#define FS 24
#define AW 26
#define SY 8
#define STR2(x) #x
#define STR(x) STR2(x)

typedef float v2f __attribute__((ext_vector_type(2)));
typedef unsigned v8u __attribute__((ext_vector_type(8)));
typedef unsigned long long u64;
typedef __attribute__((address_space(1))) u64 gu64;
typedef __attribute__((address_space(3))) unsigned lu32;

constexpr int CPG = 14, HW = 65536;
constexpr int NDW = 8, NT = 512;
constexpr int R = 2, P = 16, NSET = 16;
constexpr int BSTR = 544;
constexpr int SPIN_CAP = 1 << 20;

struct Lds {
    v2f part[R][NT];
    v2f ab[R][16];
    unsigned w[13];
};
constexpr int CNT = 0;
constexpr int RDY = 2;
constexpr int ANC = 4;
constexpr int ADJ = 12;
static_assert(__builtin_offsetof(Lds, ab) == 8192 && __builtin_offsetof(Lds, w) == 8448, "offsets used in the asm");
static_assert(D0 == 2 && D0 <= D && FS >= 28 - D && FS < AW && AW <= 27 && SY > 1 && SY + D < 27, "schedule");

template <int CTRL>
__device__ __forceinline__ float dpp(float v) {
    return __builtin_bit_cast(float, __builtin_amdgcn_update_dpp(0, __builtin_bit_cast(int, v), CTRL, 0xf, 0xf, true));
}
__device__ __forceinline__ float row_sum(float v) {
    v += dpp<0xB1>(v);
    v += dpp<0x4E>(v);
    v += dpp<0x141>(v);
    v += dpp<0x140>(v);
    return v;
}
__device__ __forceinline__ float wave_sum(float v) {
    const int b = __builtin_bit_cast(int, row_sum(v));
    return (__builtin_bit_cast(float, __builtin_amdgcn_readlane(b, 0)) + __builtin_bit_cast(float, __builtin_amdgcn_readlane(b, 16))) +
           (__builtin_bit_cast(float, __builtin_amdgcn_readlane(b, 32)) + __builtin_bit_cast(float, __builtin_amdgcn_readlane(b, 48)));
}
__device__ __forceinline__ unsigned now() { return (unsigned)__builtin_amdgcn_s_memrealtime(); }
__device__ __forceinline__ void nap(unsigned ticks) {
    const unsigned from = now();
    while (now() - from < ticks) __builtin_amdgcn_s_sleep(1);
}
#define RLX __ATOMIC_RELAXED, __HIP_MEMORY_SCOPE_AGENT
#define RLXW __ATOMIC_RELAXED, __HIP_MEMORY_SCOPE_WORKGROUP

#define V8(a) "v" #a "0", "v" #a "1", "v" #a "2", "v" #a "3", "v" #a "4", "v" #a "5", "v" #a "6", "v" #a "7", "v" #a "8", "v" #a "9"
#define CALL(l) "s_call_b64 s[52:53], .L" l "%=\n"
#define NEXTCH "s_add_u32 s51, s51, s43\n s_cmp_eq_u32 s51, s58\n s_cselect_b32 s51, 0, s51\n"
#define UNPACK(idx)                                                                                                      \
    "s_set_gpr_idx_on " idx ", gpr_idx(SRC1)\n"                                                                          \
    "v_lshlrev_b32 v112, 16, v0\n v_and_b32 v113, s55, v0\n v_lshlrev_b32 v114, 16, v1\n v_and_b32 v115, s55, v1\n"      \
    "v_lshlrev_b32 v116, 16, v2\n v_and_b32 v117, s55, v2\n v_lshlrev_b32 v118, 16, v3\n v_and_b32 v119, s55, v3\n"      \
    "s_set_gpr_idx_off\n"

__global__ __launch_bounds__(NT + 64, 3) __attribute__((aligned(4096)))
void gn_kernel(const void* __restrict__ x, void* __restrict__ y, const unsigned short* __restrict__ gam,
               const unsigned short* __restrict__ bet, u64* __restrict__ ws) {
    __shared__ Lds lds;
    unsigned t;
    u64 tc;
    const unsigned lbase = (unsigned)(size_t)(__attribute__((address_space(3))) void*)&lds;
    asm volatile(
        "s_load_dwordx2 s[40:41], %[ka], 0\n"
        "s_load_dwordx2 s[44:45], %[ka], 8\n"
        "s_getpc_b64 vcc\n.Lgn_pc:\n v_lshlrev_b32 v137, 7, v0\n v_min_u32 v137, .Lgn_end-.Lgn_pc, v137\n"
        "global_load_dword v137, v137, vcc\n"
        "v_readfirstlane_b32 s62, v0\n"
        "s_lshr_b32 s51, %[bid], 4\n"
        "s_mov_b32 s58, 0x1c0000\n"
        "v_mov_b32 %[t], v0\n"
        "s_mul_i32 s54, s51, s58\n"
        "s_and_b32 s49, %[bid], 3\n s_and_b32 s50, %[bid], 12\n s_lshl3_add_u32 s49, s50, s49\n"
        "v_and_b32 v136, 63, v0\n"
        "s_lshl4_add_u32 s49, s49, s62\n s_lshl_b32 s49, s49, 6\n"
        "s_sub_u32 s50, s51, 14\n s_cselect_b32 s51, s51, s50\n s_lshl_b32 s51, s51, 17\n"
        "v_lshl_add_u32 v136, v136, 4, s49\n"
        "s_mov_b32 s43, 0x20000\n s_lshl_b32 s42, s58, 5\n"
        ".set .Lk, 0\n"
        ".rept " STR(D0) "\n"
        "s_add_u32 s[66+.Lk], s54, s51\n" NEXTCH
        ".set .Lk, .Lk+1\n"
        ".endr\n"
        "s_cmpk_lt_u32 s62, 512\n"
        "s_waitcnt lgkmcnt(0)\n"
        "s_memrealtime s[56:57]\n"
        "s_cbranch_scc0 .Lside%=\n"
        ".set .Lk, 0\n"
        ".rept " STR(D0) "\n"
        "buffer_load_dwordx4 v[4*.Lk:4*.Lk+3], v136, s[40:43], s[66+.Lk] offen nt\n"
        ".set .Lk, .Lk+1\n"
        ".endr\n"
        "s_mov_b64 s[46:47], s[42:43]\n"
        "s_mov_b32 s61, s54\n s_lshr_b32 s62, s62, 6\n"
        "s_mov_b32 s48, 0\n s_mov_b32 s55, 0xffff0000\n s_mov_b32 s60, 0\n"
        "v_mov_b32 v133, %[lb]\n v_mov_b32 v132, 1\n v_lshl_add_u32 v134, %[t], 3, %[lb]\n v_mov_b32 v135, %[lb]\n"
        "s_lshl2_add_u32 s63, s62, %[lb]\n v_mov_b32 v125, s63\n"
        "s_add_u32 s59, %[lb], 8192\n"
        "s_movk_i32 s64, %[lp]\n"
        "s_waitcnt lgkmcnt(0)\n"
        "s_lshl_b32 s56, s56, 4\n"
        "s_sub_u32 s50, %[l0], s56\n"
        ".rept " STR(D) "-" STR(D0) "\n"
        CALL("pace")
        "buffer_load_dwordx4 v[4*.Lk:4*.Lk+3], v136, s[40:43], s49 offen nt\n"
        ".set .Lk, .Lk+1\n"
        ".endr\n"
        ".set .Li, 0\n .set .Ls, 0\n"
        ".rept 28\n"
        "s_waitcnt vmcnt(.Lk-1-.Li+.Ls)\n"
        ".if .Li == 1\n s_barrier\n .endif\n"
        ".if .Li == " STR(SY) "\n ds_read_b32 v112, v135 offset:8496\n s_waitcnt lgkmcnt(0)\n v_readfirstlane_b32 s56, v112\n s_add_u32 s64, s64, s56\n .endif\n"
        ".if .Li == 0 || .Li == 14\n"
        "v_lshlrev_b32 v124, 16, v[4*.Li]\n v_mov_b64 v[126:127], 0\n v_mov_b64 v[128:129], 0\n"
        ".endif\n"
        CALL("stats")
        ".if .Li == 13 || .Li == 27\n" CALL("part") ".endif\n"
        ".if .Lk < 28\n"
        ".if .Lk == 14\n s_add_u32 s54, s54, 0x1c00000\n s_addk_i32 s64, " STR(BEND) "*16/28\n .endif\n"
        CALL("pace")
        "buffer_load_dwordx4 v[4*.Lk:4*.Lk+3], v136, s[40:43], s49 offen nt\n"
        ".set .Lk, .Lk+1\n"
        ".elseif .Li >= " STR(FS) " && .Li < 27\n"
        ".if .Ls == 0\n s_mov_b32 s54, s61\n" CALL("ready") ".endif\n"
        CALL("store")
        ".set .Ls, .Ls+1\n"
        ".endif\n"
        ".set .Li, .Li+1\n"
        ".endr\n"
        ".La%=:\n" CALL("store") "s_cmp_lg_u32 s60, 56\n s_cbranch_scc1 .La%=\n"
        "s_add_u32 s54, s54, 0x1c00000\n s_add_u32 s59, s59, 128\n"
        CALL("ready")
        "s_sub_u32 s50, s56, 14*%[sp]\n"
        ".Lb%=:\n" CALL("store") "s_cmpk_lg_u32 s60, 112\n s_cbranch_scc1 .Lb%=\n"
        "s_endpgm\n"

        ".Lstats%=:\n"
        UNPACK("s48")
        "v_pk_add_f32 v[112:113], v[112:113], v[124:125] op_sel_hi:[1,0] neg_lo:[0,1] neg_hi:[0,1]\n"
        "v_pk_add_f32 v[114:115], v[114:115], v[124:125] op_sel_hi:[1,0] neg_lo:[0,1] neg_hi:[0,1]\n"
        "v_pk_add_f32 v[116:117], v[116:117], v[124:125] op_sel_hi:[1,0] neg_lo:[0,1] neg_hi:[0,1]\n"
        "v_pk_add_f32 v[118:119], v[118:119], v[124:125] op_sel_hi:[1,0] neg_lo:[0,1] neg_hi:[0,1]\n"
        "s_add_u32 s48, s48, 4\n"
        "v_pk_add_f32 v[126:127], v[126:127], v[112:113]\n v_pk_fma_f32 v[128:129], v[112:113], v[112:113], v[128:129]\n"
        "v_pk_add_f32 v[126:127], v[126:127], v[114:115]\n v_pk_fma_f32 v[128:129], v[114:115], v[114:115], v[128:129]\n"
        "v_pk_add_f32 v[126:127], v[126:127], v[116:117]\n v_pk_fma_f32 v[128:129], v[116:117], v[116:117], v[128:129]\n"
        "v_pk_add_f32 v[126:127], v[126:127], v[118:119]\n v_pk_fma_f32 v[128:129], v[118:119], v[118:119], v[128:129]\n"
        "s_setpc_b64 s[52:53]\n"

        ".Lpart%=:\n"
        "v_add_f32 v126, v126, v127\n v_add_f32 v128, v128, v129\n"
        "v_mul_f32 v127, 0x3c124925, v126\n"
        "v_add_f32 v112, v124, v127\n v_fma_f32 v113, -v126, v127, v128\n"
        "ds_write_b64 v134, v[112:113]\n"
        "s_mov_b64 exec, 1\n ds_add_u32 v133, v132 offset:8448\n s_mov_b64 exec, -1\n"
        "v_add_u32 v134, 0x1000, v134\n v_add_u32 v133, 4, v133\n"
        "s_setpc_b64 s[52:53]\n"

        ".Lp1%=:\n s_sleep 1\n"
        ".Lpace%=:\n s_memrealtime s[56:57]\n s_add_u32 s49, s54, s51\n s_waitcnt lgkmcnt(0)\n"
        "s_lshl4_add_u32 s56, s56, s50\n s_cmp_lt_i32 s56, 0\n s_cbranch_scc1 .Lp1%=\n"
        "s_sub_u32 s50, s50, s64\n"
        NEXTCH
        "s_setpc_b64 s[52:53]\n"

        ".Lr1%=:\n s_sleep 1\n"
        ".Lready%=:\n ds_read_b32 v112, v135 offset:8456\n s_waitcnt lgkmcnt(0)\n v_readfirstlane_b32 s56, v112\n"
        "s_cmp_eq_u32 s56, 0\n s_cbranch_scc1 .Lr1%=\n"
        "v_add_u32 v135, 4, v135\n"
        "s_setpc_b64 s[52:53]\n"

        ".Lstore%=:\n"
        "s_lshr_b32 s49, s51, 14\n s_add_u32 s49, s49, s59\n v_mov_b32 v130, s49\n ds_read_b64 v[130:131], v130\n"
        UNPACK("s60")
        "s_add_u32 s60, s60, 4\n s_add_u32 s49, s54, s51\n"
        "s_waitcnt lgkmcnt(0)\n"
        "s_memrealtime s[56:57]\n"
        "v_pk_fma_f32 v[112:113], v[112:113], v[130:131], v[130:131] op_sel:[0,0,1] op_sel_hi:[1,0,1]\n"
        "v_pk_fma_f32 v[114:115], v[114:115], v[130:131], v[130:131] op_sel:[0,0,1] op_sel_hi:[1,0,1]\n"
        "v_pk_fma_f32 v[116:117], v[116:117], v[130:131], v[130:131] op_sel:[0,0,1] op_sel_hi:[1,0,1]\n"
        "v_pk_fma_f32 v[118:119], v[118:119], v[130:131], v[130:131] op_sel:[0,0,1] op_sel_hi:[1,0,1]\n"
        "s_cmp_eq_u32 s60, 4\n"
        "v_cvt_pk_bf16_f32 v120, v112, v113\n v_cvt_pk_bf16_f32 v121, v114, v115\n"
        "v_cvt_pk_bf16_f32 v122, v116, v117\n v_cvt_pk_bf16_f32 v123, v118, v119\n"
        "s_cbranch_scc1 .Lanchor%=\n"
        ".Ls2%=:\n s_waitcnt lgkmcnt(0)\n s_lshl4_add_u32 s56, s56, s50\n s_cmp_lt_i32 s56, 0\n s_cbranch_scc1 .Ls1%=\n"
        "buffer_store_dwordx4 v[120:123], v136, s[44:47], s49 offen sc1\n"
        "s_addk_i32 s50, -%[sp]\n"
        NEXTCH
        "s_setpc_b64 s[52:53]\n"
        ".Lanchor%=:\n s_waitcnt vmcnt(27-" STR(AW) ") lgkmcnt(0)\n s_memrealtime s[56:57]\n s_waitcnt lgkmcnt(0)\n"
        "s_lshl4_add_u32 s56, s56, s50\n s_min_i32 s56, s56, 16*" STR(ANCHOR_CAP) "\n s_addk_i32 s56, 16*" STR(ANCHOR_DELAY) "\n s_sub_u32 s50, s50, s56\n"
        "v_mov_b32 v112, s50\n ds_write_b32 v125, v112 offset:8464\n"
        ".Ls1%=:\n s_sleep 1\n s_memrealtime s[56:57]\n s_branch .Ls2%=\n"

        ".Lside%=:\n"
        "s_waitcnt vmcnt(0) lgkmcnt(0)\n s_mov_b64 %[tc], s[56:57]\n"
        : [t] "=&v"(t), [tc] "=&s"(tc)
        : [ka] "s"(__builtin_amdgcn_kernarg_segment_ptr()), [bid] "s"(blockIdx.x), [lb] "s"(lbase), "{v0}"(threadIdx.x),
          [lp] "i"(LOAD_SPAN * 16 / 28), [l0] "i"(LOAD_LEAD * 16 - D0 * (LOAD_SPAN * 16 / 28)), [sp] "i"(STORE_SPAN * 16 / 28)
        : "memory", "scc", "vcc", "v1", "v2", "v3", "v4", "v5", "v6", "v7", "v8", "v9", V8(1), V8(2), V8(3), V8(4), V8(5), V8(6), V8(7),
          V8(8), V8(9), V8(10), V8(11), V8(12), "v130", "v131", "v132", "v133", "v134", "v135", "v136", "v137",
          "s40", "s41", "s42", "s43", "s44", "s45", "s46", "s47", "s48", "s49", "s50", "s51", "s52", "s53", "s54", "s55", "s56", "s57",
          "s58", "s59", "s60", "s61", "s62", "s63", "s64", "s66", "s67");

    __builtin_amdgcn_s_setprio(3);
    const int lane = t & 63;
    const unsigned set = blockIdx.x / P, j = blockIdx.x % P;
    if (lane < 13) lds.w[lane] = 0;
    gu64* board = (gu64*)ws + (size_t)set * (R * BSTR);
    if (lane == 0) __hip_atomic_store(board + 2 * j + 1, tc, RLX);
    v8u g0, b0, g1, b1;
    asm volatile("s_waitcnt lgkmcnt(0)\n s_load_dwordx8 %0, %2, %4\n s_load_dwordx8 %1, %3, %4\n s_barrier\n s_waitcnt lgkmcnt(0)"
                 : "=&s"(g0), "=&s"(b0) : "s"(gam), "s"(bet), "s"(set * (CPG * 2)) : "memory");
    gu64* peer = board + (lane & (P - 1)) * 2 + ((lane >> 4) & 1);
    const u64 w0 = __hip_atomic_load(peer, RLX);
    unsigned snap = ((unsigned)w0 & 3u) | ((unsigned)__hip_atomic_load(peer + BSTR, RLX) & 3u) << 2;
    asm volatile("s_waitcnt vmcnt(0)" : "+v"(snap) :: "memory");
    {
        const bool ok = w0 - tc + 128 < 256;
        const float dsum = row_sum(ok ? (float)(int)(w0 - tc) : 0.f), dcnt = row_sum(ok ? 1.f : 0.f);
        if (lane == 16) lds.w[ADJ] = (int)(dsum * __builtin_amdgcn_rcpf(fmaxf(dcnt, 1.f)) * (16.f / (27 - SY - D)));
    }
    asm volatile("s_load_dwordx8 %0, %2, %4\n s_load_dwordx8 %1, %3, %4\n s_waitcnt lgkmcnt(0)"
                 : "=&s"(g1), "=&s"(b1) : "s"(gam), "s"(bet), "s"((set + NSET) * (CPG * 2) - 4) : "memory");
    int gw = 0, bw = 0;
#pragma unroll
    for (int k = 0; k < CPG / 2; ++k)
        asm("v_writelane_b32 %0, %2, %6\n v_writelane_b32 %1, %3, %6\n v_writelane_b32 %0, %4, %7\n v_writelane_b32 %1, %5, %7"
            : "+v"(gw), "+v"(bw) : "s"(g0[k]), "s"(b0[k]), "s"(g1[k + 1]), "s"(b1[k + 1]), "i"(2 * k), "i"(32 + 2 * k));
    asm("s_nop 1" : "+v"(gw), "+v"(bw));
    gw = __builtin_amdgcn_update_dpp(0, gw, 0xA0, 0xf, 0xf, true);
    bw = __builtin_amdgcn_update_dpp(0, bw, 0xA0, 0xf, 0xf, true);
    const float gl = __uint_as_float(lane & 1 ? gw & 0xffff0000u : (unsigned)gw << 16);
    const float bl = __uint_as_float(lane & 1 ? bw & 0xffff0000u : (unsigned)bw << 16);
    const unsigned mine0 = __builtin_amdgcn_readlane(snap, j), mine1 = __builtin_amdgcn_readlane(snap, j + 16);
    const unsigned mine = (lane & 1) ? mine1 : mine0;
    int spin = 0;
    u64 first = 0;
#pragma nounroll
    for (int r = 0; r < R; ++r) {
        while (__builtin_amdgcn_readfirstlane(__hip_atomic_load((lu32*)&lds.w[CNT + r], RLXW)) != NDW) __builtin_amdgcn_s_sleep(1);
        asm volatile("" ::: "memory");
        const unsigned aw = lds.w[ANC + (lane & 7)], a0 = __builtin_amdgcn_readfirstlane(aw);
        const unsigned fin = a0 + (int)(row_sum((float)(int)(aw - a0)) * (1.f / 16));
        v2f p[NDW];
        float m = 0.f;
#pragma unroll
        for (int w = 0; w < NDW; ++w) { p[w] = lds.part[r][w * 64 + lane]; m += p[w].x; }
        m = wave_sum(m) * (1.f / NT);
        float q = 0.f, e = 0.f;
#pragma unroll
        for (int w = 0; w < NDW; ++w) { const float d = p[w].x - m; q += p[w].y; e = fmaf(d, d, e); }
        q = wave_sum(fmaf(e, (float)(CPG * 8), q));
        const u64 body = (lane & 1) ? (u64)fin << 32 : ((u64)__float_as_uint(m) << 32) | (__float_as_uint(q) & ~3u);
        if (lane <= 2 * r)
            __hip_atomic_store(board + (lane & 2 ? 0 : r) * BSTR + 2 * j + (lane & 1),
                               lane & 2 ? first : body | (((mine >> (2 * r)) + 1) & 3u), RLX);
        first = body | ((mine + 2) & 3u);
        u64 val;
        unsigned delay = r ? POLL_1 : POLL_0;
#pragma nounroll
        for (;; ++spin) {
            nap(delay);
            delay = POLL_NEXT;
            val = __hip_atomic_load(peer + r * BSTR, RLX);
            if (__all((((unsigned)val ^ (snap >> (2 * r))) & 3u) != 0 || (lane & 16) > 16 * r)) break;
            if (spin >= SPIN_CAP) { val = (lane & 16) ? (u64)fin << 32 : ~0ull; break; }
        }
        const float pm = __uint_as_float((unsigned)(val >> 32)), pq = __uint_as_float((unsigned)val & ~3u);
        const float mu = row_sum(pm) * (1.f / P);
        const float pd = pm - mu;
        const float M2 = row_sum(fmaf(pd * (float)(CPG * 8 * NT), pd, pq));
        const float a = gl * __builtin_amdgcn_rsqf(M2 * (1.f / (CPG * HW)) + 1e-5f);
        if ((lane >> 4) == 2 * r) lds.ab[r][lane & 15] = (v2f){a, bl - mu * a};
        const unsigned slot = fin + (int)(row_sum((float)(int)((unsigned)(val >> 32) - fin)) * (1.f / P));
        asm volatile("" ::: "memory");
        if (lane == 16) __hip_atomic_store((lu32*)&lds.w[RDY + r], r ? slot | 1u : 1u, RLXW);
    }
    asm volatile(".Lgn_end:");
}

extern "C" void kernel_launch(void* const* d_in, const int* in_sizes, int n_in, void* d_out, int out_size, void* d_ws,
                              size_t ws_size, hipStream_t stream) {
    gn_kernel<<<NSET * P, NT + 64, 0, stream>>>(d_in[0], d_out, (const unsigned short*)d_in[1], (const unsigned short*)d_in[2],
                                                (u64*)d_ws);
}
```
